```python
import torch

def _softmax(x, axis=-1):
    return torch.softmax(x, dim=axis)
def _gelu(x):
    return torch.nn.functional.gelu(x, approximate="tanh")
def _silu(x):
    return torch.nn.functional.silu(x)
def _sigmoid(x):
    return torch.sigmoid(x)
def _layernorm(x, axis=-1, eps=1e-5):
    m = x.mean(dim=axis, keepdim=True)
    v = x.var(dim=axis, keepdim=True, unbiased=False)
    return (x - m) / torch.sqrt(v + eps)
def _scatter_add(tgt, idx, src, axis=0):
    return tgt.clone().index_add_(axis, idx, src)
def _segment_sum(src, seg, n):
    out = torch.zeros((n,) + src.shape[1:], dtype=src.dtype)
    return out.index_add_(0, seg, src)
def _pad(x, pw):
    flat = tuple(v for pair in reversed(pw) for v in pair)
    return torch.nn.functional.pad(x, flat)

kernel_name = 'concat_heads__ssm_conv_h3__ssm_conv_s4__prompt'

def setup_inputs():

    _w = (lambda a: (lambda t: t.to(torch.bfloat16) if t.is_floating_point() else t)(torch.as_tensor(a)))
    def _A_setup():
        B, S, D = 1, 2048, 640
        ks = 4
        return {
            "q": _w((lambda *s: torch.randn(*s, dtype=torch.bfloat16) * 0.05)(B, S, D)),
            "k": _w((lambda *s: torch.randn(*s, dtype=torch.bfloat16) * 0.2)(B, S, D)),
            "v": _w((lambda *s: torch.randn(*s, dtype=torch.bfloat16) * 0.2)(B, S, D)),
            "shift_k": _w((lambda *s: torch.randn(*s, dtype=torch.bfloat16) * 0.2)(D, ks) * 0.5),
            "ssm_k": _w((lambda *s: torch.randn(*s, dtype=torch.bfloat16) * 0.8)(D, S) * (1.0 / S ** 0.5)),
            "ssm_D": _w((lambda *s: torch.randn(*s, dtype=torch.bfloat16) * 0.2)(D)),
        }
    _A_in = _A_setup()

    def _B_setup():
        B, L, D, N = 1, 2048, 640, 192
        A_re = -(lambda _x: torch.exp(torch.as_tensor(_x)))((lambda *s: torch.randn(*s, dtype=torch.bfloat16) * 3.2)(D, N))
        A_im = torch.pi * (lambda *s: torch.randn(*s, dtype=torch.bfloat16) * 0.05)(D, N)
        B_re = (lambda *s: torch.randn(*s, dtype=torch.bfloat16) * 0.1)(D, N)
        B_im = (lambda *s: torch.randn(*s, dtype=torch.bfloat16) * 0.1)(D, N)
        C_re = (lambda *s: torch.randn(*s, dtype=torch.bfloat16) * 0.1)(D, N)
        C_im = (lambda *s: torch.randn(*s, dtype=torch.bfloat16) * 0.1)(D, N)
        log_dt = (lambda *s: torch.randn(*s, dtype=torch.bfloat16) * 0.05)(D) * 0.5 - 4.0
        Dskip = (lambda *s: torch.randn(*s, dtype=torch.bfloat16) * 0.05)(D)
        return {
            "u": _w((lambda *s: torch.randn(*s, dtype=torch.bfloat16) * 0.05)(B, L, D)),
            "A_re": _w(A_re),
            "A_im": _w(A_im),
            "B_re": _w(B_re),
            "B_im": _w(B_im),
            "C_re": _w(C_re),
            "C_im": _w(C_im),
            "log_dt": _w(log_dt),
            "Dskip": _w(Dskip),
        }
    _B_in = _B_setup()

    return {
        "A_q": _A_in["q"],
        "A_k": _A_in["k"],
        "A_v": _A_in["v"],
        "A_shift_k": _A_in["shift_k"],
        "A_ssm_k": _A_in["ssm_k"],
        "A_ssm_D": _A_in["ssm_D"],
        "B_u": _B_in["u"],
        "B_A_re": _B_in["A_re"],
        "B_A_im": _B_in["A_im"],
        "B_B_re": _B_in["B_re"],
        "B_B_im": _B_in["B_im"],
        "B_C_re": _B_in["C_re"],
        "B_C_im": _B_in["C_im"],
        "B_log_dt": _B_in["log_dt"],
        "B_Dskip": _B_in["Dskip"],
    }

def reference(A_q, A_k, A_v, A_shift_k, A_ssm_k, A_ssm_D, B_u, B_A_re, B_A_im, B_B_re, B_B_im, B_C_re, B_C_im, B_log_dt, B_Dskip):
    def _A_ref(q, k, v, shift_k, ssm_k, ssm_D):
        B, S, D = q.shape
        ks = shift_k.shape[-1]
        k_pad = _pad(k, ((0, 0), (ks - 1, 0), (0, 0)))
        k_shift = torch.zeros_like(k)
        for t in range(ks):
            k_shift = k_shift + k_pad[:, t:t + S, :] * shift_k[:, ks - 1 - t][None, None, :]
        kv = k_shift * v
        L = 2 * S
        kv_pad = _pad(kv, ((0, 0), (0, S), (0, 0)))
        kf_pad = _pad(ssm_k, ((0, 0), (0, S)))
        kv_f = (lambda x, axes=(-2,-1): torch.fft.rfft2(x.float(), dim=axes))(kv_pad[:, None, :, :], axes=(1, 2))
        kf_f = (lambda x, axes=(-2,-1): torch.fft.rfft2(x.float(), dim=axes))(kf_pad[:, None, :], axes=(1, 2))
        kf_f = (lambda x, *d: x.permute(*d))(kf_f, 1, 2, 0)
        y_f = kv_f * kf_f[None, :, :, :]
        y = (lambda x, s, axes=(-2,-1): torch.fft.irfft2(x, s=s, dim=axes).to(torch.bfloat16))(y_f, (1, L), axes=(1, 2))[:, 0, :S, :]
        y = y + kv * ssm_D[None, None, :]
        return q * y

    def _B_ref(u, A_re, A_im, B_re, B_im, C_re, C_im, log_dt, Dskip):
        Bsz, L, D = u.shape
        dt = torch.exp(log_dt)[:, None]
        A = A_re + 1j * A_im
        Bc = B_re + 1j * B_im
        Cc = C_re + 1j * C_im
        logdA = dt * A
        dB = dt * Bc
        l = (lambda x, dt: x.to(dt))(torch.arange(L), A_re.dtype)
        P = torch.exp(logdA[:, :, None] * l[None, None, :])
        K = torch.einsum("dn,dnl,dn->dl", Cc, P, dB).real
        L2 = 2 * L
        Kp = _pad(K, ((0, 0), (0, L)))
        up = (lambda x, *d: x.permute(*d))(u, 0, 2, 1)
        up = _pad(up, ((0, 0), (0, 0), (0, L)))
        Kf = (lambda x, axes=(-2,-1): torch.fft.rfft2(x.float(), dim=axes))(Kp[:, None, :], axes=(-2, -1))
        Uf = (lambda x, axes=(-2,-1): torch.fft.rfft2(x.float(), dim=axes))(up[:, :, None, :], axes=(-2, -1))
        Yf = Uf * Kf[None, :, :, :]
        y = (lambda x, s, axes=(-2,-1): torch.fft.irfft2(x, s=s, dim=axes).to(torch.bfloat16))(Yf, (1, L2), axes=(-2, -1))[:, :, 0, :L]
        y = (lambda x, *d: x.permute(*d))(y, 0, 2, 1) + u * Dskip[None, None, :]
        return y

    a = _A_ref(A_q, A_k, A_v, A_shift_k, A_ssm_k, A_ssm_D)
    b = _B_ref(B_u, B_A_re, B_A_im, B_B_re, B_B_im, B_C_re, B_C_im, B_log_dt, B_Dskip)
    return torch.concatenate([a, b], axis=-1)
```

```cpp
#include <hip/hip_runtime.h>
#include <hip/hip_bf16.h>
#include <stdint.h>

#define S_LEN 2048
#define DCH 640
#define NST 192

__device__ __forceinline__ float bf2f(uint16_t x) { return __uint_as_float(((uint32_t)x) << 16); }
__device__ __forceinline__ uint16_t f2bf(float f) {
    __hip_bfloat16 h = __float2bfloat16(f);
    return *reinterpret_cast<uint16_t*>(&h);
}
__device__ __forceinline__ float rbf(float f) { return bf2f(f2bf(f)); }

__global__ void prep_x(const uint16_t* k, const uint16_t* v, const uint16_t* shift_k, const uint16_t* ssm_k,
                       const uint16_t* u, float* x, float* kf) {
    int idx = blockIdx.x * blockDim.x + threadIdx.x;
    if (idx >= S_LEN * DCH) return;
    int t = idx / DCH, d = idx % DCH;
    float acc = 0.f;
    for (int tt = 0; tt < 4; ++tt) {
        int s = t + tt - 3;
        float kk = s >= 0 ? bf2f(k[s * DCH + d]) : 0.f;
        float p = rbf(kk * bf2f(shift_k[d * 4 + 3 - tt]));
        acc = rbf(acc + p);
    }
    float kv = rbf(acc * bf2f(v[idx]));
    x[d * S_LEN + t] = kv;
    x[(DCH + d) * S_LEN + t] = bf2f(u[idx]);
    kf[d * S_LEN + t] = bf2f(ssm_k[d * S_LEN + t]);
}

__global__ void gen_k(const uint16_t* Are, const uint16_t* Aim, const uint16_t* Bre, const uint16_t* Bim,
                      const uint16_t* Cre, const uint16_t* Cim, const uint16_t* log_dt, float* kf) {
    int d = blockIdx.y;
    int l = blockIdx.x * blockDim.x + threadIdx.x;
    float dt = rbf(expf(bf2f(log_dt[d])));
    float lf = rbf((float)l);
    float acc = 0.f;
    for (int n = 0; n < NST; ++n) {
        int i = d * NST + n;
        float zr = dt * bf2f(Are[i]), zi = dt * bf2f(Aim[i]);
        float br = dt * bf2f(Bre[i]), bi = dt * bf2f(Bim[i]);
        float cr = bf2f(Cre[i]), ci = bf2f(Cim[i]);
        float wr = cr * br - ci * bi, wi = cr * bi + ci * br;
        float m = expf(zr * lf);
        float s, c;
        sincosf(zi * lf, &s, &c);
        acc += m * (wr * c - wi * s);
    }
    kf[(DCH + d) * S_LEN + l] = acc;
}

__global__ void conv(const float* x, const float* kf, const uint16_t* q, const uint16_t* ssm_D, const uint16_t* Dskip,
                     uint16_t* out) {
    int ch = blockIdx.x;
    __shared__ float sx[S_LEN], sk[S_LEN];
    for (int i = threadIdx.x; i < S_LEN; i += blockDim.x) { sx[i] = x[ch * S_LEN + i]; sk[i] = kf[ch * S_LEN + i]; }
    __syncthreads();
    for (int t = threadIdx.x; t < S_LEN; t += blockDim.x) {
        float acc = 0.f;
        for (int s = 0; s <= t; ++s) acc += sk[t - s] * sx[s];
        float y = rbf(acc);
        if (ch < DCH) {
            y = rbf(y + rbf(sx[t] * bf2f(ssm_D[ch])));
            y = bf2f(q[t * DCH + ch]) * y;
        } else {
            y = y + rbf(sx[t] * bf2f(Dskip[ch - DCH]));
        }
        out[t * 2 * DCH + ch] = f2bf(y);
    }
}

extern "C" void kernel_launch(void* const* d_in, const int* in_sizes, int n_in,
                              void* d_out, int out_size, void* d_ws, size_t ws_size,
                              hipStream_t stream) {
    const uint16_t* q = (const uint16_t*)d_in[0];
    const uint16_t* k = (const uint16_t*)d_in[1];
    const uint16_t* v = (const uint16_t*)d_in[2];
    const uint16_t* shift_k = (const uint16_t*)d_in[3];
    const uint16_t* ssm_k = (const uint16_t*)d_in[4];
    const uint16_t* ssm_D = (const uint16_t*)d_in[5];
    const uint16_t* u = (const uint16_t*)d_in[6];
    float* x = (float*)d_ws;
    float* kf = x + 1280 * S_LEN;
    prep_x<<<S_LEN * DCH / 256, 256, 0, stream>>>(k, v, shift_k, ssm_k, u, x, kf);
    gen_k<<<dim3(S_LEN / 256, DCH), 256, 0, stream>>>((const uint16_t*)d_in[7], (const uint16_t*)d_in[8], (const uint16_t*)d_in[9],
        (const uint16_t*)d_in[10], (const uint16_t*)d_in[11], (const uint16_t*)d_in[12], (const uint16_t*)d_in[13], kf);
    conv<<<1280, 256, 0, stream>>>(x, kf, q, ssm_D, (const uint16_t*)d_in[14], (uint16_t*)d_out);
}
```

```cpp
#include <hip/hip_runtime.h>
#include <hip/hip_bf16.h>
#include <stdint.h>

typedef uint32_t u32;
typedef uint16_t u16;
typedef __attribute__((ext_vector_type(2))) u32 u32x2;
typedef __attribute__((ext_vector_type(4))) u32 u32x4;
typedef __attribute__((ext_vector_type(4))) float f32x4;
typedef __attribute__((ext_vector_type(8))) __bf16 bf16x8;
typedef __attribute__((ext_vector_type(2))) __bf16 bf16x2;
typedef __attribute__((ext_vector_type(4))) short s16x4;
#define LDS __attribute__((address_space(3)))

#define STAMP(i)

constexpr int SEQ = 2048, DCH = 640, NST = 192;
constexpr int PCOPY = 4576, PSZ = 9152;
constexpr int QCOPY = 4224, QSZ = 8448;
constexpr int CHSZ = PSZ + QSZ;
constexpr int YROW = 1088;
constexpr int LDS_BYTES = 163840;

struct Params {
    const u16 *q, *k, *v, *shift_k, *ssm_k, *ssm_D, *u, *Are, *Aim, *Bre, *Bim, *Cre, *Cim, *log_dt, *Dskip;
    u16* out;
    unsigned long long* dbg;
};

__device__ __forceinline__ float bf_lo(u32 x) { return __uint_as_float(x << 16); }
__device__ __forceinline__ float bf_hi(u32 x) { return __uint_as_float(x & 0xffff0000u); }
__device__ __forceinline__ float bf1(u16 x) { return __uint_as_float(((u32)x) << 16); }
__device__ __forceinline__ u32 pk(float lo, float hi) {
    bf16x2 r = {(__bf16)lo, (__bf16)hi};
    return __builtin_bit_cast(u32, r);
}
__device__ __forceinline__ u32 shr1(u32 old, u32 src) {
    return (u32)__builtin_amdgcn_update_dpp((int)old, (int)src, 0x138, 0xf, 0xf, false);
}
__device__ __forceinline__ void bar() { asm volatile("s_waitcnt lgkmcnt(0)\n\ts_barrier" ::: "memory"); }
__device__ __forceinline__ auto rsrc(const void* p, u32 bytes) {
    return __builtin_amdgcn_make_buffer_rsrc((void*)p, 0, bytes, 0x00020000);
}
__device__ __forceinline__ void st32(LDS char* p, u32 v) { *(LDS u32*)p = v; }
__device__ __forceinline__ void st16(LDS char* p, u32 v) { *(LDS u16*)p = (u16)v; }
__device__ __forceinline__ void st64(LDS char* p, u32 a, u32 b) { *(LDS u32x2*)p = u32x2{a, b}; }
__device__ __forceinline__ void st128(LDS char* p, u32x4 v) { *(LDS u32x4*)p = v; }
__device__ __forceinline__ u32x4 ld128(const LDS char* p) { return *(const LDS u32x4*)p; }
__device__ __forceinline__ u32x2 ld64v(const LDS char* p) { return *(const volatile LDS u32x2*)p; }
__device__ __forceinline__ f32x4 mfma(u32x4 a, u32x4 b, f32x4 c) {
    return __builtin_amdgcn_mfma_f32_16x16x32_bf16(__builtin_bit_cast(bf16x8, a), __builtin_bit_cast(bf16x8, b), c, 0, 0, 0);
}

struct cf { float r, i; };
__device__ __forceinline__ cf cmul(cf a, cf b) { return cf{a.r * b.r - a.i * b.i, a.r * b.i + a.i * b.r}; }
__device__ __forceinline__ cf cexp_l(float a2, float fr, float l) {
    float m = __builtin_amdgcn_exp2f(a2 * l);
    float p = __builtin_amdgcn_fractf(fr * l);
    return cf{m * __builtin_amdgcn_cosf(p), m * __builtin_amdgcn_sinf(p)};
}

template <int S>
__device__ __forceinline__ void conv_stage(const LDS char* pP, const LDS char* pQ, f32x4 (&acc)[8]) {
    constexpr int NG = 8 - 2 * S;
#pragma unroll
    for (int rho = 0; rho < 8; ++rho) {
        u32x4 A[2], G[NG];
#pragma unroll
        for (int w = 0; w < 2; ++w) {
            const int j = rho + 8 * (2 * S + w);
            u32x2 lo = ld64v(pQ + 64 * (63 - j)), hi = ld64v(pQ + 64 * (63 - j) + 8);
            A[w] = u32x4{lo.x, lo.y, hi.x, hi.y};
        }
#pragma unroll
        for (int d = 0; d < NG; ++d) G[d] = ld128(pP + 64 * (8 * d - rho + 7));
#pragma unroll
        for (int d = 0; d < NG; ++d) {
            acc[2 * S + d] = mfma(A[0], G[d], acc[2 * S + d]);
            if (d + 1 < NG) acc[2 * S + 1 + d] = mfma(A[1], G[d], acc[2 * S + 1 + d]);
        }
    }
}

__device__ __forceinline__ void conv_wave(LDS char* ch, LDS char* stg, int ystride, int lane) {
    const int a = lane & 15, g = lane >> 4;
    const LDS char* pP = ch + (a & 1) * PCOPY + 64 * (a >> 1) + 16 * g;
    const LDS char* pQ = ch + PSZ + (a & 1) * QCOPY + 56 - 8 * (a >> 1) + 16 * g;
    const int n = a >> 1;
    LDS char* pY = stg + 4 * ((16 * n + 4 * g + (a & 1)) ^ ((n >> 2) << 3));
    f32x4 acc[8];
#pragma unroll
    for (int c = 0; c < 8; ++c) acc[c] = f32x4{0.f, 0.f, 0.f, 0.f};
#define STAGE(S)                                                                   \
    conv_stage<S>(pP, pQ, acc);                                                    \
    _Pragma("unroll") for (int e = 0; e < 2; ++e) {                                \
        LDS char* y = pY + (S & 1) * ystride + e * 512;                            \
        st32(y, pk(acc[2 * S + e][0], acc[2 * S + e][1]));                         \
        st32(y + 8, pk(acc[2 * S + e][2], acc[2 * S + e][3]));                     \
    }                                                                              \
    bar();
    STAGE(0) STAGE(1) STAGE(2) STAGE(3)
#undef STAGE
}

__device__ __forceinline__ void zero_pads(LDS char* ch, int lane) {
    if (lane < 28) { st128(ch + 16 * lane, u32x4{0, 0, 0, 0}); st128(ch + PCOPY + 16 * lane, u32x4{0, 0, 0, 0}); }
    if (lane < 33) { st16(ch + PSZ + 4094 + 2 * lane, 0); st16(ch + PSZ + QCOPY + 4096 + 2 * lane, 0); }
}

__device__ __forceinline__ s16x4 tr_read(const LDS char* p) {
    typedef __attribute__((ext_vector_type(4))) short v4;
    return __builtin_amdgcn_ds_read_tr16_b64_v4i16((LDS v4*)p);
}

__device__ __forceinline__ void a_conv(const Params& p, LDS char* lds, int ch0, int w, int lane) {
    LDS char* ch = lds + w * CHSZ;
    const int c = ch0 + w;
    auto rs = rsrc(p.ssm_k, SEQ * DCH * 2);
    u32x4 kk[4];
#pragma unroll
    for (int it = 0; it < 4; ++it) kk[it] = __builtin_amdgcn_raw_buffer_load_b128(rs, c * 4096 + (it * 64 + lane) * 16, 0, 2);
    const float dd = bf1(p.ssm_D[c]);
    zero_pads(ch, lane);
    if (lane == 0) kk[0].x = (kk[0].x & 0xffff0000u) | (pk(bf_lo(kk[0].x) + dd, 0.f) & 0xffffu);
#pragma unroll
    for (int it = 0; it < 4; ++it) {
        u32 old = it == 0 ? 0u : (u32)__builtin_amdgcn_readlane((int)kk[it - 1].w, 63);
        u32 pw = shr1(old, kk[it].w);
        st128(ch + PCOPY + 448 + (it * 64 + lane) * 16, kk[it]);
        st128(ch + 448 + (it * 64 + lane) * 16, u32x4{pw, kk[it].x, kk[it].y, kk[it].z});
    }
    STAMP(1);
    bar();
    STAMP(2);
    conv_wave(ch, lds + 8 * CHSZ + w * YROW, 8 * YROW, lane);
}

struct KV { u32x4 k0, k1, v0, v1, h1, h2, h3; };

__device__ __forceinline__ void a_help(const Params& p, LDS char* lds, int ch0, int hw, int lane) {
    auto rk = rsrc(p.k, SEQ * DCH * 2), rv = rsrc(p.v, SEQ * DCH * 2), rq = rsrc(p.q, SEQ * DCH * 2);
    auto ro = rsrc(p.out, SEQ * DCH * 4);
    const int col = ch0 * 2;
    auto load_kv = [&](int s) {
        KV r;
        const int base = (512 * s + 128 * hw) * 1280 + col;
        const int o = base + lane * 2560;
        r.k0 = __builtin_amdgcn_raw_buffer_load_b128(rk, o, 0, 0);
        r.k1 = __builtin_amdgcn_raw_buffer_load_b128(rk, o + 1280, 0, 0);
        r.v0 = __builtin_amdgcn_raw_buffer_load_b128(rv, o, 0, 0);
        r.v1 = __builtin_amdgcn_raw_buffer_load_b128(rv, o + 1280, 0, 0);
        const bool neg = (s == 0 && hw == 0);
        r.h1 = __builtin_amdgcn_raw_buffer_load_b128(rk, neg ? 0x7ffffff0 : base - 1280, 0, 0);
        r.h2 = __builtin_amdgcn_raw_buffer_load_b128(rk, neg ? 0x7ffffff0 : base - 2560, 0, 0);
        r.h3 = __builtin_amdgcn_raw_buffer_load_b128(rk, neg ? 0x7ffffff0 : base - 3840, 0, 0);
        return r;
    };
    KV b0 = load_kv(0), b1 = load_kv(1);
    float wt[8][4];
#pragma unroll
    for (int c = 0; c < 8; ++c) {
        u32x2 x = *(const u32x2*)(p.shift_k + (ch0 + c) * 4);
        wt[c][0] = bf_lo(x.x); wt[c][1] = bf_hi(x.x); wt[c][2] = bf_lo(x.y); wt[c][3] = bf_hi(x.y);
    }
    auto build = [&](const KV& r, int s) {
        const int t0 = 512 * s + 128 * hw + 2 * lane;
        LDS char* q1 = lds + PSZ + QCOPY + 4092 - 2 * t0;
        LDS char* q0 = lds + PSZ + 4090 - 2 * t0;
#pragma unroll
        for (int d = 0; d < 4; ++d) {
            const u32 m1 = shr1(r.h1[d], r.k1[d]), m2 = shr1(r.h2[d], r.k0[d]), m3 = shr1(r.h3[d], m1);
#pragma unroll
            for (int e = 0; e < 2; ++e) {
                const int c = 2 * d + e;
                const float k0 = e ? bf_hi(r.k0[d]) : bf_lo(r.k0[d]), k1 = e ? bf_hi(r.k1[d]) : bf_lo(r.k1[d]);
                const float a1 = e ? bf_hi(m1) : bf_lo(m1), a2 = e ? bf_hi(m2) : bf_lo(m2), a3 = e ? bf_hi(m3) : bf_lo(m3);
                const float v0 = e ? bf_hi(r.v0[d]) : bf_lo(r.v0[d]), v1 = e ? bf_hi(r.v1[d]) : bf_lo(r.v1[d]);
                const float s0 = wt[c][0] * k0 + (wt[c][1] * a1 + (wt[c][2] * a2 + wt[c][3] * a3));
                const float s1 = wt[c][0] * k1 + (wt[c][1] * k0 + (wt[c][2] * a1 + wt[c][3] * a2));
                const u32 x = pk(s1 * v1, s0 * v0);
                st32(q1 + c * CHSZ, x);
                st16(q0 + c * CHSZ, x);
                st16(q0 + c * CHSZ + 2, x >> 16);
            }
        }
    };
    struct QQ { u32x4 a, b; };
    auto load_q = [&](int s) {
        QQ r;
        const int o = (512 * s + 128 * hw + lane) * 1280 + col;
        r.a = __builtin_amdgcn_raw_buffer_load_b128(rq, o, 0, 0);
        r.b = __builtin_amdgcn_raw_buffer_load_b128(rq, o + 64 * 1280, 0, 0);
        return r;
    };
    const int chunk = (128 * hw + (lane & 48)) / 4 + (lane & 3);
    auto yaddr = [&](int ck) { return lds + 8 * CHSZ + ((lane >> 2) & 3) * YROW + 8 * (ck ^ (((ck >> 5) & 1) << 2)); };
    const LDS char* y0 = yaddr(chunk);
    const LDS char* y1 = yaddr(chunk + 16);
    auto store = [&](const QQ& qq, int s) {
#pragma unroll
        for (int h = 0; h < 2; ++h) {
            const LDS char* y = (h ? y1 : y0) + (s & 1) * 8 * YROW;
            u32x2 lo = __builtin_bit_cast(u32x2, tr_read(y)), hi = __builtin_bit_cast(u32x2, tr_read(y + 4 * YROW));
            const u32x4 qv = h ? qq.b : qq.a;
            const u32x4 yv = {lo.x, lo.y, hi.x, hi.y};
            u32x4 o;
#pragma unroll
            for (int d = 0; d < 4; ++d) o[d] = pk(bf_lo(yv[d]) * bf_lo(qv[d]), bf_hi(yv[d]) * bf_hi(qv[d]));
            __builtin_amdgcn_raw_buffer_store_b128(o, ro, (512 * s + 128 * hw + 64 * h + lane) * 2560 + col, 0, 0);
        }
    };
    build(b0, 0);
    b0 = load_kv(2);
    QQ q0 = load_q(0);
    STAMP(1);
    bar();
    build(b1, 1);
    b1 = load_kv(3);
    QQ q1 = load_q(1);
    bar();
    store(q0, 0);
    q0 = load_q(2);
    build(b0, 2);
    bar();
    store(q1, 1);
    q1 = load_q(3);
    build(b1, 3);
    bar();
    store(q0, 2);
    bar();
    store(q1, 3);
}

constexpr int B_STG = 4 * CHSZ;
constexpr int B_SCR = B_STG + 8 * YROW;

__device__ __forceinline__ void kgen(const Params& p, LDS char* scr, int c, int pass, int lane, f32x4 (&D)[3]) {
    const int idx = c * NST + pass * 64 + lane;
    const float are = bf1(p.Are[idx]), aim = bf1(p.Aim[idx]), bre = bf1(p.Bre[idx]), bim = bf1(p.Bim[idx]);
    const float cre = bf1(p.Cre[idx]), cim = bf1(p.Cim[idx]);
    const float dt = bf_lo(pk(expf(bf1(p.log_dt[c])), 0.f));
    const float a2 = (dt * are) * 1.4426950408889634f, fr = (dt * aim) * 0.15915494309189535f;
    const cf W = cmul(cf{cre, cim}, cf{dt * bre, dt * bim});
    const cf E1 = cexp_l(a2, fr, 1.f), E8 = cexp_l(a2, fr, 8.f), E128 = cexp_l(a2, fr, 128.f);
    const cf E2 = cmul(E1, E1), E4 = cmul(E2, E2);
    LDS char* w0 = scr + 4 * lane;
    LDS char* w1 = w0 + PCOPY;
    const LDS char* r0 = scr + (lane & 15) * 288 + (lane >> 4) * 16;
    const LDS char* r1 = r0 + PCOPY;
    auto put = [&](LDS char* b, int row, cf x) { st32(b + row * 288, pk(x.r, x.i)); };
    {
        const cf Ec = {E8.r, -E8.i}, Ec2 = cmul(Ec, Ec);
        cf xe = {1.f, 0.f}, xo = Ec;
#pragma unroll
        for (int j = 0; j < 8; ++j) {
            put(w0, 2 * j, xe); put(w0, 2 * j + 1, xo);
            if (j < 7) { xe = j == 0 ? Ec2 : cmul(xe, Ec2); xo = cmul(xo, Ec2); }
        }
    }
    u32x4 Vf[4];
#pragma unroll
    for (int kk = 0; kk < 4; ++kk) Vf[kk] = ld128(r0 + 64 * kk);
    cf X[8];
    X[0] = W;
#pragma unroll
    for (int i = 1; i < 8; ++i) X[i] = cmul(X[i - 1], E128);
    {
        cf xa = X[0], xb = X[1];
#pragma unroll
        for (int b = 0; b < 8; ++b) {
            put(w1, b, xa); put(w1, 8 + b, xb);
            if (b < 7) { xa = cmul(xa, E1); xb = cmul(xb, E1); }
        }
    }
#pragma unroll
    for (int kk = 0; kk < 4; ++kk) D[0] = mfma(ld128(r1 + 64 * kk), Vf[kk], D[0]);
    {
        cf xa = X[2], xb = X[3];
#pragma unroll
        for (int b = 0; b < 4; ++b) {
            put(w0, b, xa); put(w0, 4 + b, xb);
            if (b < 3) { xa = cmul(xa, E2); xb = cmul(xb, E2); }
        }
#pragma unroll
        for (int i = 0; i < 4; ++i) { put(w0, 8 + 2 * i, X[4 + i]); put(w0, 9 + 2 * i, cmul(X[4 + i], E4)); }
    }
#pragma unroll
    for (int kk = 0; kk < 4; ++kk) D[1] = mfma(ld128(r0 + 64 * kk), Vf[kk], D[1]);
    {
        const cf E256 = cmul(E128, E128);
        cf xa = X[6], xb = X[7];
#pragma unroll
        for (int G = 0; G < 4; ++G) {
            xa = cmul(xa, E256); xb = cmul(xb, E256);
            put(w1, 4 * G, xa); put(w1, 4 * G + 1, xb);
        }
        put(w1, 2, cmul(xa, E256));
    }
#pragma unroll
    for (int kk = 0; kk < 4; ++kk) D[2] = mfma(ld128(r1 + 64 * kk), Vf[kk], D[2]);
}

__device__ __forceinline__ u32 lolo(u32 a, u32 b) { return __builtin_amdgcn_perm(b, a, 0x05040100); }

__device__ __forceinline__ void b_expand(const Params& p, LDS char* ch, LDS char* s1, LDS char* s2, int c, int lane, f32x4 (&D)[3]) {
#pragma unroll
    for (int t = 0; t < 3; ++t) {
        f32x4 x = *(const LDS f32x4*)(s1 + t * 1024 + lane * 16), y = *(const LDS f32x4*)(s2 + t * 1024 + lane * 16);
        D[t] = D[t] + x + y;
    }
    const int j = lane & 15, G = lane >> 4;
    if (lane == 0) D[0][0] += bf1(p.Dskip[c]);
    zero_pads(ch, lane);
    if (lane == 0) st32(ch + 448, 0);
    LDS char* P0 = ch + 452;
    LDS char* P1 = ch + PCOPY + 448;
    {
        const int L = 128 * (G >> 1) + 8 * j + 4 * (G & 1);
        const u32 d0 = pk(D[0][0], D[0][1]), d1 = pk(D[0][2], D[0][3]);
        st64(P1 + 2 * L, d0, d1);
        st32(P0 + 2 * L, d0); st32(P0 + 2 * L + 4, d1);
    }
    const u32 c01 = pk(D[1][0], D[1][1]), c23 = pk(D[1][2], D[1][3]), a01 = pk(D[2][0], D[2][1]), a2 = pk(D[2][2], 0.f);
    LDS char* Ka = s1 + 3072;
    const int mc0 = G < 2 ? 32 + 16 * G + j : 64 + 32 * (G - 2) + j;
    const int ma0 = 128 + 32 * G + j;
    st16(Ka + 2 * mc0, c01);
    if (G >= 2) st16(Ka + 2 * mc0 + 32, c23);
    st16(Ka + 2 * ma0, a01);
    st16(Ka + 2 * ma0 + 32, a01 >> 16);
    if (lane == 0) st16(Ka + 512, a2);
    const u32 bc0 = *(volatile LDS u16*)(Ka + 2 * mc0 + 2), bc1 = *(volatile LDS u16*)(Ka + 2 * mc0 + 34);
    const u32 ba0 = *(volatile LDS u16*)(Ka + 2 * ma0 + 2), ba1 = *(volatile LDS u16*)(Ka + 2 * ma0 + 34);
    auto chunk = [&](int m, u32x4 v) {
        st128(P1 + 16 * m, v);
        st32(P0 + 16 * m, v.x); st32(P0 + 16 * m + 4, v.y); st32(P0 + 16 * m + 8, v.z); st32(P0 + 16 * m + 12, v.w);
    };
    if (G < 2) {
        chunk(mc0, u32x4{lolo(c01, c01), lolo(c01 >> 16, c23), lolo(c23, c23), lolo(c23 >> 16, bc0)});
    } else {
        chunk(mc0, u32x4{lolo(c01, c01), c01, lolo(c01 >> 16, c01 >> 16), lolo(bc0, bc0)});
        chunk(mc0 + 16, u32x4{lolo(c23, c23), c23, lolo(c23 >> 16, c23 >> 16), lolo(bc1, bc1)});
    }
    {
        const u32 x0 = a01 & 0xffffu, x1 = a01 >> 16;
        const u32 aa0 = lolo(x0, x0), bb0 = lolo(ba0, ba0), aa1 = lolo(x1, x1), bb1 = lolo(ba1, ba1);
        chunk(ma0, u32x4{aa0, aa0, (j & 1) ? bb0 : lolo(x0, ba0), bb0});
        chunk(ma0 + 16, u32x4{aa1, aa1, (j & 1) ? bb1 : lolo(x1, ba1), bb1});
    }
}

__device__ __forceinline__ void b_main(const Params& p, LDS char* lds, int ch0, int w, int lane) {
    const int cw = w & 3, pass = w >> 2;
    auto ru = rsrc(p.u, SEQ * DCH * 2);
    auto ro = rsrc(p.out, SEQ * DCH * 4);
    const int col = ch0 * 2;
    u32x2 ub[4][2];
    if (pass == 2) {
#pragma unroll
        for (int s = 0; s < 4; ++s) {
            const int o = (512 * s + 128 * cw + 2 * lane) * 1280 + col;
            ub[s][0] = __builtin_amdgcn_raw_buffer_load_b64(ru, o, 0, 0);
            ub[s][1] = __builtin_amdgcn_raw_buffer_load_b64(ru, o + 1280, 0, 0);
        }
    }
    LDS char* scr = pass == 0 ? lds + cw * CHSZ : lds + B_SCR + (w - 4) * PSZ;
    f32x4 D[3] = {{0.f, 0.f, 0.f, 0.f}, {0.f, 0.f, 0.f, 0.f}, {0.f, 0.f, 0.f, 0.f}};
    kgen(p, scr, ch0 + cw, pass, lane, D);
    if (pass != 0) {
#pragma unroll
        for (int t = 0; t < 3; ++t) *(LDS f32x4*)(scr + t * 1024 + lane * 16) = D[t];
    }
    STAMP(1);
    bar();
    if (pass == 0) {
        b_expand(p, lds + cw * CHSZ, lds + B_SCR + cw * PSZ, lds + B_SCR + (4 + cw) * PSZ, ch0 + cw, lane, D);
        bar();
        STAMP(2);
        conv_wave(lds + cw * CHSZ, lds + B_STG + cw * YROW, 4 * YROW, lane);
    } else if (pass == 2) {
        auto build = [&](int s) {
            const int t0 = 512 * s + 128 * cw + 2 * lane;
            LDS char* q1 = lds + PSZ + QCOPY + 4092 - 2 * t0;
            LDS char* q0 = lds + PSZ + 4090 - 2 * t0;
#pragma unroll
            for (int c = 0; c < 4; ++c) {
                const u32 x = __builtin_amdgcn_perm(ub[s][0][c >> 1], ub[s][1][c >> 1], (c & 1) ? 0x07060302 : 0x05040100);
                st32(q1 + c * CHSZ, x);
                st16(q0 + c * CHSZ, x);
                st16(q0 + c * CHSZ + 2, x >> 16);
            }
        };
        const int chunk = (128 * cw + (lane & 48)) / 4 + (lane & 3);
        auto yaddr = [&](int ck) { return lds + B_STG + ((lane >> 2) & 3) * YROW + 8 * (ck ^ (((ck >> 5) & 1) << 2)); };
        const LDS char* y0 = yaddr(chunk);
        const LDS char* y1 = yaddr(chunk + 16);
        auto store = [&](int s) {
#pragma unroll
            for (int h = 0; h < 2; ++h) {
                u32x2 y = __builtin_bit_cast(u32x2, tr_read((h ? y1 : y0) + (s & 1) * 4 * YROW));
                __builtin_amdgcn_raw_buffer_store_b64(y, ro, (512 * s + 128 * cw + 64 * h + lane) * 2560 + 1280 + col, 0, 0);
            }
        };
        build(0);
        bar();
        build(1);
        bar();
        store(0); build(2);
        bar();
        store(1); build(3);
        bar();
        store(2);
        bar();
        store(3);
    }
}

__global__ __launch_bounds__(768) void h3s4_kernel(Params p) {
    __shared__ __attribute__((aligned(16))) char smem[LDS_BYTES];
    LDS char* lds = (LDS char*)smem;
    const int w = __builtin_amdgcn_readfirstlane(threadIdx.x >> 6), lane = threadIdx.x & 63;
    const int xcd = blockIdx.x & 7, slot = blockIdx.x >> 3;
    STAMP(0);
    if (slot < 10) {
        const int ch0 = (xcd * 10 + slot) * 8;
        if (w < 8) a_conv(p, lds, ch0, w, lane);
        else a_help(p, lds, ch0, w - 8, lane);
    } else if (slot < 30) {
        b_main(p, lds, (xcd * 20 + slot - 10) * 4, w, lane);
    }
    STAMP(3);
}

extern "C" void kernel_launch(void* const* d_in, const int* in_sizes, int n_in,
                              void* d_out, int out_size, void* d_ws, size_t ws_size,
                              hipStream_t stream) {
    Params p;
    p.q = (const u16*)d_in[0]; p.k = (const u16*)d_in[1]; p.v = (const u16*)d_in[2];
    p.shift_k = (const u16*)d_in[3]; p.ssm_k = (const u16*)d_in[4]; p.ssm_D = (const u16*)d_in[5];
    p.u = (const u16*)d_in[6]; p.Are = (const u16*)d_in[7]; p.Aim = (const u16*)d_in[8];
    p.Bre = (const u16*)d_in[9]; p.Bim = (const u16*)d_in[10]; p.Cre = (const u16*)d_in[11];
    p.Cim = (const u16*)d_in[12]; p.log_dt = (const u16*)d_in[13]; p.Dskip = (const u16*)d_in[14];
    p.out = (u16*)d_out;
    p.dbg = (unsigned long long*)d_ws;
    h3s4_kernel<<<256, 768, 0, stream>>>(p);
}
```

```cpp
#include <hip/hip_runtime.h>
#include <hip/hip_bf16.h>
#include <stdint.h>

typedef uint32_t u32;
typedef uint16_t u16;
typedef __attribute__((ext_vector_type(2))) u32 u32x2;
typedef __attribute__((ext_vector_type(4))) u32 u32x4;
typedef __attribute__((ext_vector_type(4))) float f32x4;
typedef __attribute__((ext_vector_type(2))) float f32x2;
typedef __attribute__((ext_vector_type(8))) __bf16 bf16x8;
typedef __attribute__((ext_vector_type(2))) __bf16 bf16x2;
typedef __attribute__((ext_vector_type(4))) short s16x4;
#define LDS __attribute__((address_space(3)))

#define STAMP(i)
#define STAMP_INIT()
#define STAMP_DUMP()

constexpr int SEQ = 2048, DCH = 640, NST = 192;
constexpr int PCOPY = 4576, PSZ = 9152;
constexpr int QCOPY = 4224, QSZ = 8448;
constexpr int CHSZ = PSZ + QSZ;
constexpr int YROW = 1088;
constexpr int LDS_BYTES = 163840;

struct Params {
    const u16 *q, *k, *v, *shift_k, *ssm_k, *ssm_D, *u, *Are, *Aim, *Bre, *Bim, *Cre, *Cim, *log_dt, *Dskip;
    u16* out;
    unsigned long long* dbg;
};

#if defined(__HIP_DEVICE_COMPILE__)
#pragma clang attribute push(__attribute__((target("no-packed-fp32-ops"))), apply_to = function)
#endif
__device__ __forceinline__ float bf_lo(u32 x) { return __builtin_bit_cast(float, (u32)(x << 16)); }
__device__ __forceinline__ float bf_hi(u32 x) { return __builtin_bit_cast(float, (u32)(x & 0xffff0000u)); }
__device__ __forceinline__ float bf1(u16 x) { return __builtin_bit_cast(float, (u32)(((u32)x) << 16)); }
__device__ __forceinline__ u32 pk(float lo, float hi) {
    bf16x2 r = {(__bf16)lo, (__bf16)hi};
    return __builtin_bit_cast(u32, r);
}
__device__ __forceinline__ u32 shr1(u32 old, u32 src) {
    return (u32)__builtin_amdgcn_update_dpp((int)old, (int)src, 0x138, 0xf, 0xf, false);
}
__device__ __forceinline__ void bar() { asm volatile("s_waitcnt lgkmcnt(0)\n\ts_barrier" ::: "memory"); }
__device__ __forceinline__ auto rsrc(const void* p, u32 bytes) {
    return __builtin_amdgcn_make_buffer_rsrc((void*)p, 0, bytes, 0x00020000);
}
__device__ __forceinline__ void st32(LDS char* p, u32 v) { *(LDS u32*)p = v; }
__device__ __forceinline__ void st16(LDS char* p, u32 v) { *(volatile LDS u16*)p = (u16)v; }
__device__ __forceinline__ void st64(LDS char* p, u32 a, u32 b) { *(LDS u32x2*)p = u32x2{a, b}; }
__device__ __forceinline__ void st128(LDS char* p, u32x4 v) { *(LDS u32x4*)p = v; }
__device__ __forceinline__ u32x4 ld128(const LDS char* p) { return *(const LDS u32x4*)p; }
__device__ __forceinline__ u32x2 ld64v(const LDS char* p) { return *(const volatile LDS u32x2*)p; }
__device__ __forceinline__ f32x4 mfma(u32x4 a, u32x4 b, f32x4 c) {
    return __builtin_amdgcn_mfma_f32_16x16x32_bf16(__builtin_bit_cast(bf16x8, a), __builtin_bit_cast(bf16x8, b), c, 0, 0, 0);
}

struct cf { float r, i; };
__device__ __forceinline__ cf cmul(cf a, cf b) { return cf{__builtin_fmaf(-a.i, b.i, a.r * b.r), __builtin_fmaf(a.i, b.r, a.r * b.i)}; }
__device__ __forceinline__ cf cexp_l(float a2, float fr, float l) {
    float m = __builtin_amdgcn_exp2f(a2 * l);
    float p = __builtin_amdgcn_fractf(fr * l);
    return cf{m * __builtin_amdgcn_cosf(p), m * __builtin_amdgcn_sinf(p)};
}

#define NOB 0
#define NOCONV 0
#define PF 1
#define PFAUX 2
__device__ __forceinline__ u32 probe(const Params& p, int id, int lane) {
    const int kind = (0xD4u >> id) & 1, qtr = (0xED84u >> (2 * id)) & 3;
    const int xcd = blockIdx.x & 7, slot = blockIdx.x >> 3, grp = lane >> 4;
    const bool lowx = xcd < 4;
    const int xl = 8 + ((xcd & 3) >> 1);
    const u16* base; int line = xcd; bool on = true;
    if (kind == 0) {
        base = grp == 0 ? p.k : grp == 1 ? p.v : lowx ? (grp == 2 ? p.k : p.v) : p.u;
        if (grp == 3 || (grp == 2 && lowx)) line = xl;
    } else {
        base = grp == 1 ? p.u : p.q;
        if (grp == 2) line = xl;
        on = grp == 0 || (lowx && grp < 3);
    }
    const int row = 512 * qtr + 16 * slot + (lane & 15);
    u32 r = 0;
    if (PF && on) r = __builtin_nontemporal_load((const u32*)(base + row * 640 + line * 64));
    return r;
}
__device__ __forceinline__ void keep(u32 x) { asm volatile("" :: "v"(x)); }

template <int S>
__device__ __forceinline__ void conv_load(const LDS char* pP, const LDS char* pQ, int rho, u32x4 (&A)[2], u32x4 (&G)[8]) {
    constexpr int NG = 8 - 2 * S;
#pragma unroll
    for (int w = 0; w < 2; ++w) {
        const int j = rho + 8 * (2 * S + w);
        u32x2 lo = ld64v(pQ + 64 * (63 - j)), hi = ld64v(pQ + 64 * (63 - j) + 8);
        A[w] = u32x4{lo.x, lo.y, hi.x, hi.y};
    }
#pragma unroll
    for (int d = 0; d < NG; ++d) G[d] = ld128(pP + 64 * (8 * d - rho + 7));
}
template <int S>
__device__ __forceinline__ void conv_mma(const u32x4 (&A)[2], const u32x4 (&G)[8], f32x4 (&acc)[8]) {
    constexpr int NG = 8 - 2 * S;
#pragma unroll
    for (int d = 0; d < NG; ++d) {
        acc[2 * S + d] = mfma(A[0], G[d], acc[2 * S + d]);
        if (d + 1 < NG) acc[2 * S + 1 + d] = mfma(A[1], G[d], acc[2 * S + 1 + d]);
    }
}
template <int S>
__device__ __forceinline__ void conv_sched() {
    constexpr int NG = 8 - 2 * S, NR = NG + 4, NM = 2 * NG - 1;
#pragma unroll
    for (int i = 0; i < (NM > NR ? NM : NR); ++i) {
        if (i < NM) __builtin_amdgcn_sched_group_barrier(0x8, 1, 0);
        if (i < NR) __builtin_amdgcn_sched_group_barrier(0x100, 1, 0);
    }
}
template <int S>
__device__ __forceinline__ void conv_stage(const LDS char* pP, const LDS char* pQ, f32x4 (&acc)[8]) {
    u32x4 A0[2], G0[8], A1[2], G1[8];
    conv_load<S>(pP, pQ, 0, A0, G0);
    __builtin_amdgcn_sched_barrier(0);
#pragma unroll
    for (int rho = 0; rho < 8; rho += 2) {
        conv_load<S>(pP, pQ, rho + 1, A1, G1);
        conv_mma<S>(A0, G0, acc);
        conv_sched<S>();
        __builtin_amdgcn_sched_barrier(0);
        if (rho + 2 < 8) conv_load<S>(pP, pQ, rho + 2, A0, G0);
        conv_mma<S>(A1, G1, acc);
        if (rho + 2 < 8) conv_sched<S>();
        __builtin_amdgcn_sched_barrier(0);
    }
}

__device__ __forceinline__ void conv_wave(const Params& p, LDS char* ch, LDS char* stg, int ystride, int lane) {
    const int a = lane & 15, g = lane >> 4;
    const LDS char* pP = ch + (a & 1) * PCOPY + 64 * (a >> 1) + 16 * g;
    const LDS char* pQ = ch + PSZ + (a & 1) * QCOPY + 56 - 8 * (a >> 1) + 16 * g;
    const int n = a >> 1;
    LDS char* pY = stg + 4 * ((16 * n + 4 * g + (a & 1)) ^ ((n >> 2) << 3));
    f32x4 acc[8];
#pragma unroll
    for (int c = 0; c < 8; ++c) acc[c] = f32x4{0.f, 0.f, 0.f, 0.f};
#define STAGE(S)                                                                   \
    if (!NOCONV) conv_stage<S>(pP, pQ, acc);                                                    \
    _Pragma("unroll") for (int e = 0; e < 2; ++e) {                                \
        LDS char* y = pY + (S & 1) * ystride + e * 512;                            \
        st32(y, pk(acc[2 * S + e][0], acc[2 * S + e][1]));                         \
        st32(y + 8, pk(acc[2 * S + e][2], acc[2 * S + e][3]));                     \
    }                                                                              \
    STAMP(4 + 2 * S);                                                              \
    bar();                                                                         \
    STAMP(5 + 2 * S);
    STAGE(0) STAGE(1) STAGE(2) STAGE(3)
#undef STAGE
}

__device__ __forceinline__ void zero_pads(LDS char* ch, int lane) {
    if (lane < 28) { st128(ch + 16 * lane, u32x4{0, 0, 0, 0}); st128(ch + PCOPY + 16 * lane, u32x4{0, 0, 0, 0}); }
    if (lane < 33) { st16(ch + PSZ + 4094 + 2 * lane, 0); st16(ch + PSZ + QCOPY + 4096 + 2 * lane, 0); }
}

__device__ __forceinline__ s16x4 tr_read(const LDS char* p) {
    typedef __attribute__((ext_vector_type(4))) short v4;
    return __builtin_amdgcn_ds_read_tr16_b64_v4i16((LDS v4*)p);
}

__device__ __forceinline__ void a_conv(const Params& p, LDS char* lds, int ch0, int w, int lane) {
    LDS char* ch = lds + w * CHSZ;
    const int c = ch0 + w;
    auto rs = rsrc(p.ssm_k, SEQ * DCH * 2);
    u32x4 kk[4];
#pragma unroll
    for (int it = 0; it < 4; ++it) kk[it] = __builtin_amdgcn_raw_buffer_load_b128(rs, c * 4096 + (it * 64 + lane) * 16, 0, 2);
    const float dd = bf1(p.ssm_D[c]);
    zero_pads(ch, lane);
    keep(kk[0].x);
    const u32 pf0 = w >= 2 ? probe(p, w, lane) : 0u;
    if (lane == 0) kk[0].x = (kk[0].x & 0xffff0000u) | (pk(bf_lo(kk[0].x) + dd, 0.f) & 0xffffu);
#pragma unroll
    for (int it = 0; it < 4; ++it) {
        u32 old = it == 0 ? 0u : (u32)__builtin_amdgcn_readlane((int)kk[it - 1].w, 63);
        u32 pw = shr1(old, kk[it].w);
        st128(ch + PCOPY + 448 + (it * 64 + lane) * 16, kk[it]);
        st128(ch + 448 + (it * 64 + lane) * 16, u32x4{pw, kk[it].x, kk[it].y, kk[it].z});
    }
    STAMP(1);
    bar();
    STAMP(2);
    conv_wave(p, ch, lds + 8 * CHSZ + w * YROW, 8 * YROW, lane);
    keep(pf0);
}

struct KV { u32x4 k0, k1, v0, v1, h; };
typedef const __attribute__((address_space(4))) u32* cptr;

__device__ __forceinline__ void a_help(const Params& p, LDS char* lds, int ch0, int hw, int lane, u32 pf) {
    __builtin_amdgcn_s_setprio(3);
    auto rk = rsrc(p.k, SEQ * DCH * 2), rv = rsrc(p.v, SEQ * DCH * 2), rq = rsrc(p.q, SEQ * DCH * 2);
    auto ro = rsrc(p.out, SEQ * DCH * 4);
    const int col = ch0 * 2;
    auto load_kv = [&](int s) {
        KV r;
        const int base = (512 * s + 128 * hw) * 1280 + col;
        const int o = base + lane * 2560;
        r.k0 = __builtin_amdgcn_raw_buffer_load_b128(rk, o, 0, 0);
        r.k1 = __builtin_amdgcn_raw_buffer_load_b128(rk, o + 1280, 0, 0);
        const bool off = lane >= 3 || (s == 0 && hw == 0);
        r.h = __builtin_amdgcn_raw_buffer_load_b128(rk, off ? 0x7ffffff0 : base - 1280 * (lane + 1), 0, 0);
        r.v0 = __builtin_amdgcn_raw_buffer_load_b128(rv, o, 0, 0);
        r.v1 = __builtin_amdgcn_raw_buffer_load_b128(rv, o + 1280, 0, 0);
        return r;
    };
    struct QQ { u32x4 a, b; };
    auto load_q = [&](int s) {
        QQ r;
        const int o = (512 * s + 128 * hw + lane) * 1280 + col;
        r.a = __builtin_amdgcn_raw_buffer_load_b128(rq, o, 0, 0);
        r.b = __builtin_amdgcn_raw_buffer_load_b128(rq, o + 64 * 1280, 0, 0);
        return r;
    };
    float wt[8][4];
    {
        cptr sw = (cptr)(p.shift_k + ch0 * 4);
#pragma unroll
        for (int c = 0; c < 8; ++c) {
            const u32 x = sw[2 * c], y = sw[2 * c + 1];
            wt[c][0] = bf_lo(x); wt[c][1] = bf_hi(x); wt[c][2] = bf_lo(y); wt[c][3] = bf_hi(y);
        }
    }
    keep(pf);
    STAMP(12);
    KV b0 = load_kv(0), b1 = load_kv(1);
    auto build = [&](const KV& r, int s) {
        const int t0 = 512 * s + 128 * hw + 2 * lane;
        LDS char* q1 = lds + PSZ + QCOPY + 4092 - 2 * t0;
        LDS char* q0 = lds + PSZ + 4090 - 2 * t0;
#pragma unroll
        for (int d = 0; d < 4; ++d) {
            const u32 h2 = (u32)__builtin_amdgcn_update_dpp(0, (int)r.h[d], 0x101, 0xf, 0xf, true);
            const u32 h3 = (u32)__builtin_amdgcn_update_dpp(0, (int)r.h[d], 0x102, 0xf, 0xf, true);
            const u32 m1 = shr1(r.h[d], r.k1[d]), m2 = shr1(h2, r.k0[d]), m3 = shr1(h3, m1);
#pragma unroll
            for (int e = 0; e < 2; ++e) {
                const int c = 2 * d + e;
                const float k0 = e ? bf_hi(r.k0[d]) : bf_lo(r.k0[d]), k1 = e ? bf_hi(r.k1[d]) : bf_lo(r.k1[d]);
                const float a1 = e ? bf_hi(m1) : bf_lo(m1), a2 = e ? bf_hi(m2) : bf_lo(m2), a3 = e ? bf_hi(m3) : bf_lo(m3);
                const float v0 = e ? bf_hi(r.v0[d]) : bf_lo(r.v0[d]), v1 = e ? bf_hi(r.v1[d]) : bf_lo(r.v1[d]);
                const float s0 = wt[c][0] * k0 + (wt[c][1] * a1 + (wt[c][2] * a2 + wt[c][3] * a3));
                const float s1 = wt[c][0] * k1 + (wt[c][1] * k0 + (wt[c][2] * a1 + wt[c][3] * a2));
                const u32 x = pk(s1 * v1, s0 * v0);
                st32(q1 + c * CHSZ, x);
                st16(q0 + c * CHSZ, x);
                st16(q0 + c * CHSZ + 2, x >> 16);
            }
        }
    };
    const int chunk = (128 * hw + (lane & 48)) / 4 + (lane & 3);
    auto yaddr = [&](int ck) { return lds + 8 * CHSZ + ((lane >> 2) & 3) * YROW + 8 * (ck ^ (((ck >> 5) & 1) << 2)); };
    const LDS char* y0 = yaddr(chunk);
    const LDS char* y1 = yaddr(chunk + 16);
    auto store = [&](const QQ& qq, int s) {
#pragma unroll
        for (int h = 0; h < 2; ++h) {
            const LDS char* y = (h ? y1 : y0) + (s & 1) * 8 * YROW;
            u32x2 lo = __builtin_bit_cast(u32x2, tr_read(y)), hi = __builtin_bit_cast(u32x2, tr_read(y + 4 * YROW));
            const u32x4 qv = h ? qq.b : qq.a;
            const u32x4 yv = {lo.x, lo.y, hi.x, hi.y};
            u32x4 o;
#pragma unroll
            for (int d = 0; d < 4; ++d) o[d] = pk(bf_lo(yv[d]) * bf_lo(qv[d]), bf_hi(yv[d]) * bf_hi(qv[d]));
            __builtin_amdgcn_raw_buffer_store_b128(o, ro, (512 * s + 128 * hw + 64 * h + lane) * 2560 + col, 0, 0);
        }
    };
    build(b0, 0);
    STAMP(1);
    bar();
    b0 = load_kv(2);
    build(b1, 1);
    STAMP(13);
    b1 = load_kv(3);
    QQ q0 = load_q(0);
    build(b0, 2);
    QQ q1 = load_q(1);
    STAMP(4);
    bar();
    store(q0, 0);
    STAMP(14);
    build(b1, 3);
    q0 = load_q(2);
    QQ q3 = load_q(3);
    STAMP(6);
    bar();
    store(q1, 1);
    STAMP(8);
    bar();
    store(q0, 2);
    STAMP(10);
    bar();
    store(q3, 3);
}

constexpr int B_STG = 4 * CHSZ;
constexpr int B_SCR = B_STG + 8 * YROW;

template <class F>
__device__ __forceinline__ void kgen(const Params& p, LDS char* scr, int c, int pass, int lane, f32x4 (&D)[3], F mid) {
    const int idx = c * NST + pass * 64 + lane;
    const float are = bf1(p.Are[idx]), aim = bf1(p.Aim[idx]), bre = bf1(p.Bre[idx]), bim = bf1(p.Bim[idx]);
    const float cre = bf1(p.Cre[idx]), cim = bf1(p.Cim[idx]);
    const float dt = bf_lo(pk(expf(bf1(p.log_dt[c])), 0.f));
    const float a2 = (dt * are) * 1.4426950408889634f, fr = (dt * aim) * 0.15915494309189535f;
    const cf W = cmul(cf{cre, cim}, cf{dt * bre, dt * bim});
    const cf E1 = cexp_l(a2, fr, 1.f), E8 = cexp_l(a2, fr, 8.f), E128 = cexp_l(a2, fr, 128.f);
    const cf E2 = cmul(E1, E1), E4 = cmul(E2, E2);
    LDS char* w0 = scr + 4 * lane;
    LDS char* w1 = w0 + PCOPY;
    const LDS char* r0 = scr + (lane & 15) * 288 + (lane >> 4) * 16;
    const LDS char* r1 = r0 + PCOPY;
    auto put = [&](LDS char* b, int row, cf x) { st32(b + row * 288, pk(x.r, x.i)); };
    {
        const cf Ec = {E8.r, -E8.i}, Ec2 = cmul(Ec, Ec);
        cf xe = {1.f, 0.f}, xo = Ec;
#pragma unroll
        for (int j = 0; j < 8; ++j) {
            put(w0, 2 * j, xe); put(w0, 2 * j + 1, xo);
            if (j < 7) { xe = j == 0 ? Ec2 : cmul(xe, Ec2); xo = cmul(xo, Ec2); }
        }
    }
    mid();
    u32x4 Vf[4];
#pragma unroll
    for (int kk = 0; kk < 4; ++kk) Vf[kk] = ld128(r0 + 64 * kk);
    cf X[8];
    X[0] = W;
#pragma unroll
    for (int i = 1; i < 8; ++i) X[i] = cmul(X[i - 1], E128);
    {
        cf xa = X[0], xb = X[1];
#pragma unroll
        for (int b = 0; b < 8; ++b) {
            put(w1, b, xa); put(w1, 8 + b, xb);
            if (b < 7) { xa = cmul(xa, E1); xb = cmul(xb, E1); }
        }
    }
#pragma unroll
    for (int kk = 0; kk < 4; ++kk) D[0] = mfma(ld128(r1 + 64 * kk), Vf[kk], D[0]);
    {
        cf xa = X[2], xb = X[3];
#pragma unroll
        for (int b = 0; b < 4; ++b) {
            put(w0, b, xa); put(w0, 4 + b, xb);
            if (b < 3) { xa = cmul(xa, E2); xb = cmul(xb, E2); }
        }
#pragma unroll
        for (int i = 0; i < 4; ++i) { put(w0, 8 + 2 * i, X[4 + i]); put(w0, 9 + 2 * i, cmul(X[4 + i], E4)); }
    }
#pragma unroll
    for (int kk = 0; kk < 4; ++kk) D[1] = mfma(ld128(r0 + 64 * kk), Vf[kk], D[1]);
    {
        const cf E256 = cmul(E128, E128);
        cf xa = X[6], xb = X[7];
#pragma unroll
        for (int G = 0; G < 4; ++G) {
            xa = cmul(xa, E256); xb = cmul(xb, E256);
            put(w1, 4 * G, xa); put(w1, 4 * G + 1, xb);
        }
        put(w1, 2, cmul(xa, E256));
    }
#pragma unroll
    for (int kk = 0; kk < 4; ++kk) D[2] = mfma(ld128(r1 + 64 * kk), Vf[kk], D[2]);
}

__device__ __forceinline__ u32 lolo(u32 a, u32 b) { return __builtin_amdgcn_perm(b, a, 0x05040100); }

__device__ __forceinline__ void b_expand(float dskip, LDS char* ch, LDS char* s1, LDS char* s2, int lane, f32x4 (&D)[3]) {
#pragma unroll
    for (int t = 0; t < 3; ++t) {
        f32x4 x = *(const LDS f32x4*)(s1 + t * 1024 + lane * 16), y = *(const LDS f32x4*)(s2 + t * 1024 + lane * 16);
        D[t] = D[t] + x + y;
    }
    const int j = lane & 15, G = lane >> 4;
    if (lane == 0) D[0][0] += dskip;
    zero_pads(ch, lane);
    if (lane == 0) st32(ch + 448, 0);
    LDS char* P0 = ch + 452;
    LDS char* P1 = ch + PCOPY + 448;
    {
        const int L = 128 * (G >> 1) + 8 * j + 4 * (G & 1);
        const u32 d0 = pk(D[0][0], D[0][1]), d1 = pk(D[0][2], D[0][3]);
        st64(P1 + 2 * L, d0, d1);
        st32(P0 + 2 * L, d0); st32(P0 + 2 * L + 4, d1);
    }
    const u32 c01 = pk(D[1][0], D[1][1]), c23 = pk(D[1][2], D[1][3]), a01 = pk(D[2][0], D[2][1]), a2 = pk(D[2][2], 0.f);
    LDS char* Ka = s1 + 3072;
    const int mc0 = G < 2 ? 32 + 16 * G + j : 64 + 32 * (G - 2) + j;
    const int ma0 = 128 + 32 * G + j;
    st16(Ka + 2 * mc0, c01);
    if (G >= 2) st16(Ka + 2 * mc0 + 32, c23);
    st16(Ka + 2 * ma0, a01);
    st16(Ka + 2 * ma0 + 32, a01 >> 16);
    if (lane == 0) st16(Ka + 512, a2);
    const u32 bc0 = *(volatile LDS u16*)(Ka + 2 * mc0 + 2), bc1 = *(volatile LDS u16*)(Ka + 2 * mc0 + 34);
    const u32 ba0 = *(volatile LDS u16*)(Ka + 2 * ma0 + 2), ba1 = *(volatile LDS u16*)(Ka + 2 * ma0 + 34);
    auto chunk = [&](int m, u32x4 v) {
        st128(P1 + 16 * m, v);
        st32(P0 + 16 * m, v.x); st32(P0 + 16 * m + 4, v.y); st32(P0 + 16 * m + 8, v.z); st32(P0 + 16 * m + 12, v.w);
    };
    if (G < 2) {
        chunk(mc0, u32x4{lolo(c01, c01), lolo(c01 >> 16, c23), lolo(c23, c23), lolo(c23 >> 16, bc0)});
    } else {
        chunk(mc0, u32x4{lolo(c01, c01), c01, lolo(c01 >> 16, c01 >> 16), lolo(bc0, bc0)});
        chunk(mc0 + 16, u32x4{lolo(c23, c23), c23, lolo(c23 >> 16, c23 >> 16), lolo(bc1, bc1)});
    }
    {
        const u32 x0 = a01 & 0xffffu, x1 = a01 >> 16;
        const u32 aa0 = lolo(x0, x0), bb0 = lolo(ba0, ba0), aa1 = lolo(x1, x1), bb1 = lolo(ba1, ba1);
        chunk(ma0, u32x4{aa0, aa0, (j & 1) ? bb0 : lolo(x0, ba0), bb0});
        chunk(ma0 + 16, u32x4{aa1, aa1, (j & 1) ? bb1 : lolo(x1, ba1), bb1});
    }
}

__device__ __forceinline__ void b_main(const Params& p, LDS char* lds, int ch0, int w, int lane) {
    const int cw = w & 3, pass = w >> 2;
    auto ru = rsrc(p.u, SEQ * DCH * 2);
    auto ro = rsrc(p.out, SEQ * DCH * 4);
    const int col = ch0 * 2;
    u32x2 ub[4][2];
    auto load_u = [&](int s) {
        const int o = (512 * s + 128 * cw + 2 * lane) * 1280 + col;
        ub[s][0] = __builtin_amdgcn_raw_buffer_load_b64(ru, o, 0, 0);
        ub[s][1] = __builtin_amdgcn_raw_buffer_load_b64(ru, o + 1280, 0, 0);
    };
    const float dskip = bf1(p.Dskip[ch0 + cw]);
    u32 pf1 = 0;
    LDS char* scr = pass == 0 ? lds + cw * CHSZ : lds + B_SCR + (w - 4) * PSZ;
    f32x4 D[3] = {{0.f, 0.f, 0.f, 0.f}, {0.f, 0.f, 0.f, 0.f}, {0.f, 0.f, 0.f, 0.f}};
    kgen(p, scr, ch0 + cw, pass, lane, D, [&] {
        if (w >= 2 && w < 8) pf1 = probe(p, w, lane);
        if (pass == 2) { load_u(0); load_u(1); }
    });
    if (pass != 0) {
#pragma unroll
        for (int t = 0; t < 3; ++t) *(LDS f32x4*)(scr + t * 1024 + lane * 16) = D[t];
    }
    STAMP(1);
    bar();
    if (pass == 0) {
        b_expand(dskip, lds + cw * CHSZ, lds + B_SCR + cw * PSZ, lds + B_SCR + (4 + cw) * PSZ, lane, D);
        bar();
        STAMP(2);
        conv_wave(p, lds + cw * CHSZ, lds + B_STG + cw * YROW, 4 * YROW, lane);
    } else if (pass == 2) {
        __builtin_amdgcn_s_setprio(3);
        auto build = [&](int s) {
            const int t0 = 512 * s + 128 * cw + 2 * lane;
            LDS char* q1 = lds + PSZ + QCOPY + 4092 - 2 * t0;
            LDS char* q0 = lds + PSZ + 4090 - 2 * t0;
#pragma unroll
            for (int c = 0; c < 4; ++c) {
                const u32 x = __builtin_amdgcn_perm(ub[s][0][c >> 1], ub[s][1][c >> 1], (c & 1) ? 0x07060302 : 0x05040100);
                st32(q1 + c * CHSZ, x);
                st16(q0 + c * CHSZ, x);
                st16(q0 + c * CHSZ + 2, x >> 16);
            }
        };
        const int chunk = (128 * cw + (lane & 48)) / 4 + (lane & 3);
        auto yaddr = [&](int ck) { return lds + B_STG + ((lane >> 2) & 3) * YROW + 8 * (ck ^ (((ck >> 5) & 1) << 2)); };
        const LDS char* y0 = yaddr(chunk);
        const LDS char* y1 = yaddr(chunk + 16);
        auto store = [&](int s) {
#pragma unroll
            for (int h = 0; h < 2; ++h) {
                u32x2 y = __builtin_bit_cast(u32x2, tr_read((h ? y1 : y0) + (s & 1) * 4 * YROW));
                __builtin_amdgcn_raw_buffer_store_b64(y, ro, (512 * s + 128 * cw + 64 * h + lane) * 2560 + 1280 + col, 0, 0);
            }
        };
        build(0);
        load_u(2); load_u(3);
        STAMP(2);
        bar();
        build(1);
        STAMP(4);
        bar();
        store(0); build(2);
        STAMP(6);
        bar();
        store(1); build(3);
        STAMP(8);
        bar();
        store(2);
        STAMP(10);
        bar();
        store(3);
    }
    keep(pf1);
}

__global__ __launch_bounds__(768) void h3s4_kernel(Params p) {
    __shared__ __attribute__((aligned(16))) char smem[LDS_BYTES];
    LDS char* lds = (LDS char*)smem;
    const int w = __builtin_amdgcn_readfirstlane(threadIdx.x >> 6), lane = threadIdx.x & 63;
    const int xcd = blockIdx.x & 7, slot = blockIdx.x >> 3;
    STAMP_INIT();
    STAMP(0);
    u32 pf = 0;
    if (w < 2 || w >= 8) pf = probe(p, w == 1 ? 1 : 0, lane);
    const int xo = 512 + (xcd & 3) * 32;
    if (slot < 8 || (xcd < 4 && slot >= 24 && slot < 28)) {
        const int ch0 = slot < 8 ? xcd * 64 + slot * 8 : xo + (slot - 24) * 8;
        if (w < 8) a_conv(p, lds, ch0, w, lane);
        else a_help(p, lds, ch0, w - 8, lane, pf);
    } else if ((slot < 24 || xcd >= 4) && !NOB) {
        b_main(p, lds, slot < 24 ? xcd * 64 + (slot - 8) * 4 : xo + (slot - 24) * 4, w, lane);
    } else {
        keep(pf);
        if (w >= 2 && w < 8) pf = probe(p, w, lane);
    }
    keep(pf);
    asm volatile("s_waitcnt vmcnt(0)" ::: "memory");
    STAMP(3);
    STAMP_DUMP();
}

#if defined(__HIP_DEVICE_COMPILE__)
#pragma clang attribute pop
#endif
extern "C" void kernel_launch(void* const* d_in, const int* in_sizes, int n_in,
                              void* d_out, int out_size, void* d_ws, size_t ws_size,
                              hipStream_t stream) {
    Params p;
    p.q = (const u16*)d_in[0]; p.k = (const u16*)d_in[1]; p.v = (const u16*)d_in[2];
    p.shift_k = (const u16*)d_in[3]; p.ssm_k = (const u16*)d_in[4]; p.ssm_D = (const u16*)d_in[5];
    p.u = (const u16*)d_in[6]; p.Are = (const u16*)d_in[7]; p.Aim = (const u16*)d_in[8];
    p.Bre = (const u16*)d_in[9]; p.Bim = (const u16*)d_in[10]; p.Cre = (const u16*)d_in[11];
    p.Cim = (const u16*)d_in[12]; p.log_dt = (const u16*)d_in[13]; p.Dskip = (const u16*)d_in[14];
    p.out = (u16*)d_out;
    p.dbg = (unsigned long long*)d_ws;
    h3s4_kernel<<<256, 768, 0, stream>>>(p);
}
```

```cpp
#include <hip/hip_runtime.h>
#include <hip/hip_bf16.h>
#include <stdint.h>

typedef uint32_t u32;
typedef uint16_t u16;
typedef __attribute__((ext_vector_type(2))) u32 u32x2;
typedef __attribute__((ext_vector_type(4))) u32 u32x4;
typedef __attribute__((ext_vector_type(2))) float f32x2;
typedef __attribute__((ext_vector_type(4))) float f32x4;
typedef __attribute__((ext_vector_type(8))) __bf16 bf16x8;
typedef __attribute__((ext_vector_type(2))) __bf16 bf16x2;
#define LDS __attribute__((address_space(3)))
typedef const __attribute__((address_space(4))) u32* cptr;

constexpr int SEQ = 2048, DCH = 640, NST = 192;
constexpr int PCOPY = 4576, PSZ = 9216;
constexpr int QCOPY = 4224, QSZ = 8512;
constexpr int CHSZ = PSZ + QSZ;
constexpr int SCR = 4 * CHSZ, SCRSZ = 8704;
constexpr int FLG = 8 * CHSZ;
constexpr int DUMP = FLG + 64;
constexpr int LDS_BYTES = 163840;

typedef unsigned long long u64;
struct Params { u64 q, k, v, shift_k, ssm_k, ssm_D, u, Are, Aim, Bre, Bim, Cre, Cim, log_dt, Dskip, out; };
typedef __attribute__((address_space(1))) u16* g16;

__device__ __forceinline__ float bf_lo(u32 x) { return __builtin_bit_cast(float, (u32)(x << 16)); }
__device__ __forceinline__ float bf_hi(u32 x) { return __builtin_bit_cast(float, (u32)(x & 0xffff0000u)); }
__device__ __forceinline__ u32 pk(float lo, float hi) {
    bf16x2 r = {(__bf16)lo, (__bf16)hi};
    return __builtin_bit_cast(u32, r);
}
__device__ __forceinline__ u32 shr1(u32 old, u32 src) {
    return (u32)__builtin_amdgcn_update_dpp((int)old, (int)src, 0x138, 0xf, 0xf, false);
}
__device__ __forceinline__ void bar() { asm volatile("s_waitcnt lgkmcnt(0)\n\ts_barrier" ::: "memory"); }
__device__ __forceinline__ auto rsrc(u64 p, u32 bytes) {
    return __builtin_amdgcn_make_buffer_rsrc((__attribute__((address_space(1))) void*)p, 0, bytes, 0x00020000);
}
__device__ __forceinline__ float uni16(u64 p, u32 i) {
    const u32 x = *(cptr)(p + ((2 * i) & ~3u));
    return __builtin_bit_cast(float, (x >> (16 * (i & 1))) << 16);
}
__device__ __forceinline__ void st32(LDS char* p, u32 v) { *(LDS u32*)p = v; }
__device__ __forceinline__ void st16(LDS char* p, u32 v) { *(volatile LDS u16*)p = (u16)v; }
__device__ __forceinline__ void st64(LDS char* p, u32 a, u32 b) { *(LDS u32x2*)p = u32x2{a, b}; }
__device__ __forceinline__ void st128(LDS char* p, u32x4 v) { *(LDS u32x4*)p = v; }
__device__ __forceinline__ u32x4 ld128(const LDS char* p) { return *(const LDS u32x4*)p; }
__device__ __forceinline__ u32x2 ld64v(const LDS char* p) { return *(const volatile LDS u32x2*)p; }
__device__ __forceinline__ f32x4 mfma(u32x4 a, u32x4 b, f32x4 c) {
    return __builtin_amdgcn_mfma_f32_16x16x32_bf16(__builtin_bit_cast(bf16x8, a), __builtin_bit_cast(bf16x8, b), c, 0, 0, 0);
}
__device__ __forceinline__ u32x2 tr_read(const LDS char* p) {
    typedef __attribute__((ext_vector_type(4))) short v4;
    return __builtin_bit_cast(u32x2, __builtin_amdgcn_ds_read_tr16_b64_v4i16((LDS v4*)p));
}

__device__ __forceinline__ void finish(const Params& p) {
    __builtin_amdgcn_endpgm();
}

__device__ __forceinline__ u32 flag_ld(int i) { return *(volatile LDS u32*)(uintptr_t)(FLG + 4 * i); }
template <int SL_ = 1>
__device__ __forceinline__ void flag_wait(int i, u32 n, u32 first) {
    if ((u32)__builtin_amdgcn_readfirstlane(first) < n) {
#pragma unroll 1
        for (int it = 0; it < (1 << 20); ++it) {
            if ((u32)__builtin_amdgcn_readfirstlane(flag_ld(i)) >= n) break;
            __builtin_amdgcn_s_sleep(SL_);
        }
    }
    asm volatile("" ::: "memory");
}
template <int N = 0>
__device__ __forceinline__ void flag_add(int i) {
    asm volatile("s_waitcnt lgkmcnt(%2)\n\ts_mov_b64 exec, 1\n\tds_add_u32 %0, %1\n\ts_mov_b64 exec, -1"
                 :: "v"(FLG + 4 * i), "v"(1u), "i"(N) : "memory");
}

__device__ __forceinline__ void warm_l2(const Params& p, u32 lane) {
    const u32 xcd = blockIdx.x & 7, j = (blockIdx.x >> 3) - 8, hb = j >> 3;
    if (j >= 16) return;
    const bool lowx = xcd < 4;
    const u32 own = 128 * xcd, oth = 128 * (8 + ((xcd >> 1) & 1));
    const u64 kv = hb ? p.v : p.k;
    const u64 s0 = kv + own, s1 = lowx ? kv + oth : p.u + (hb ? oth : own), s2 = p.u + own, s3 = p.q + (hb ? oth : own);
    const u32 row = (64 * (j & 7) + lane) * 1280;
    asm volatile("s_mov_b32 m0, %0" :: "s"(DUMP + 64) : "memory");
    auto go = [&](u64 b, u32 qtr) { asm volatile("global_load_lds_dword %0, %1" :: "v"(row + qtr * 512 * 1280), "s"(b) : "memory"); };
    asm volatile("s_nop 0");
    go(s0, 0); go(s1, 0);
    asm volatile("s_barrier" ::: "memory");
    __builtin_amdgcn_s_sleep(36);
    go(s0, 1); go(s1, 1);
    if (lowx) go(s2, hb);
    __builtin_amdgcn_s_sleep(40);
    go(s0, 2); go(s1, 2); go(s0, 3); go(s1, 3);
    if (lowx) go(s2, 2 + hb);
    if (lowx || !hb) { go(s3, 0); go(s3, 1); go(s3, 2); go(s3, 3); }
}

__device__ __forceinline__ u32x4 ld128v(const LDS char* p) { return *(const volatile LDS u32x4*)p; }
__device__ __forceinline__ void mfma_acc(f32x4& c, u32x2 a0, u32x2 a1, u32x4 b) {
    const u32x4 a = {a0.x, a0.y, a1.x, a1.y};
    asm volatile("v_mfma_f32_16x16x32_bf16 %0, %1, %2, %0" : "+v"(c) : "v"(a), "v"(b));
}
constexpr int HOLD = 1;
constexpr int LAG = 10;
constexpr int NA = 16, NG = 24;
struct Sched {
    int nm, nr;
    short acc[288], a[288], g[288];
    short off[288], slot[288], kind[288];
    short upto[289];
    short first[5], need[5];
};
constexpr Sched make_sched(int M) {
    Sched s{};
    short use[288] = {};
    int m = 0, r = 0, na = 0, ng = 0;
    for (int S = 0; S < 4; ++S) {
        s.first[S] = m; s.need[S] = r;
        for (int rho = 0; rho < 8; ++rho) {
            int as[2] = {-1, -1};
            for (int d = 0; d < 8 - 2 * S; ++d) {
                int g = -1;
                for (int b = 0; b < 2; ++b) {
                    if (!((M >> (2 * S + d + b)) & 1)) continue;
                    if (as[b] < 0) {
                        as[b] = na++ % NA;
                        for (int h = 0; h < 2; ++h) { s.off[r] = 64 * (63 - rho - 16 * S - 8 * b) + 8 * h; s.slot[r] = as[b]; s.kind[r] = h; use[r++] = m; }
                    }
                    if (g < 0) { g = ng++ % NG; s.off[r] = 64 * (7 - rho) + 512 * d; s.slot[r] = g; s.kind[r] = 2; use[r++] = m; }
                    s.acc[m] = 2 * S + d + b; s.a[m] = as[b]; s.g[m] = g; ++m;
                }
            }
        }
    }
    s.nm = m; s.nr = r; s.first[4] = m; s.need[4] = r;
    r = 0;
    while (use[r] < LAG) ++r;
    for (int i = 0; i <= s.nm; ++i) {
        s.upto[i] = r;
        for (int k = 0; k < 2 && r < s.nr && use[r] <= i + LAG + 1 - k; ++k) ++r;
    }
    return s;
}
template <int M> constexpr Sched SC = make_sched(M);
struct Frags { u32x2 a[NA][2]; u32x4 g[NG]; };
template <int M, int R0, int R1>
__device__ __forceinline__ void frag_loads(const LDS char* pP, const LDS char* pQ, Frags& F, u32& fl) {
    if constexpr (R0 < R1) {
        constexpr int S = R0 == SC<M>.need[1] ? 1 : R0 == SC<M>.need[2] ? 2 : R0 == SC<M>.need[3] ? 3 : 0;
        constexpr int P = M < 4 ? 0 : R0 + 12 == SC<M>.need[1] ? 1 : R0 + 12 == SC<M>.need[2] ? 2 : R0 + 12 == SC<M>.need[3] ? 3 : 0;
        if constexpr (S > 0) flag_wait(S, 4, fl);
        if constexpr (P > 0) fl = flag_ld(P);
        if constexpr (SC<M>.kind[R0] < 2) F.a[SC<M>.slot[R0]][SC<M>.kind[R0]] = ld64v(pQ + SC<M>.off[R0]);
        else F.g[SC<M>.slot[R0]] = ld128v(pP + SC<M>.off[R0]);
        frag_loads<M, R0 + 1, R1>(pP, pQ, F, fl);
    }
}
template <int M, int R0, int R1, bool FILTER>
__device__ __forceinline__ void first_loads(const LDS char* pP, const LDS char* pQ, Frags& F, u32& fl) {
    if constexpr (R0 < R1) {
        if constexpr ((SC<M>.kind[R0] == 2) == FILTER) frag_loads<M, R0, R0 + 1>(pP, pQ, F, fl);
        first_loads<M, R0 + 1, R1, FILTER>(pP, pQ, F, fl);
    }
}
template <int S, bool NOP>
__device__ __forceinline__ void put_y(LDS char* pY, f32x4 (&acc)[8]) {
#pragma unroll
    for (int e = 0; e < 2; ++e) {
        if (NOP) asm volatile("s_nop 7" : "+v"(acc[2 * S + e]));
        else asm volatile("" : "+v"(acc[2 * S + e]));
        LDS char* y = pY + 1024 * (3 - S) + (e ? 0 : QCOPY);
        *(volatile LDS u32*)y = pk(acc[2 * S + e][0], acc[2 * S + e][1]);
        *(volatile LDS u32*)(y + 8) = pk(acc[2 * S + e][2], acc[2 * S + e][3]);
    }
}
template <int M, int M0, int M1>
__device__ __forceinline__ void conv_mms(const Params& p, const LDS char* pP, const LDS char* pQ, LDS char* pY,
                                         Frags& F, f32x4 (&acc)[8], u32& fl) {
    if constexpr (M0 + 1 < M1) {
        conv_mms<M, M0, (M0 + M1) / 2>(p, pP, pQ, pY, F, acc, fl);
        conv_mms<M, (M0 + M1) / 2, M1>(p, pP, pQ, pY, F, acc, fl);
    } else {
        mfma_acc(acc[SC<M>.acc[M0]], F.a[SC<M>.a[M0]][0], F.a[SC<M>.a[M0]][1], F.g[SC<M>.g[M0]]);
        frag_loads<M, SC<M>.upto[M0], SC<M>.upto[M0 + 1]>(pP, pQ, F, fl);
        if constexpr (M0 >= HOLD) asm volatile("" :: "v"(F.a[SC<M>.a[M0 - HOLD]][0]), "v"(F.a[SC<M>.a[M0 - HOLD]][1]), "v"(F.g[SC<M>.g[M0 - HOLD]]));
        constexpr int S = M0 >= SC<M>.first[3] ? 3 : M0 >= SC<M>.first[2] ? 2 : M0 >= SC<M>.first[1] ? 1 : 0;
        constexpr bool fin = S > 0 && ((M >> (2 * S - 2)) & 1);
        if constexpr (S > 0 && S < 3 && M0 == SC<M>.first[S]) __builtin_amdgcn_s_setprio(2 - S);
        if constexpr (fin && M0 == SC<M>.first[S] + 2) put_y<(S > 0 ? S - 1 : 0), false>(pY, acc);
        if constexpr (fin && M0 == SC<M>.first[S] + 8) {
            flag_add<SC<M>.upto[M0 + 1] - SC<M>.upto[SC<M>.first[S] + 3]>(3 + S);
        }
    }
}
template <int M>
__device__ __forceinline__ void conv_run(const Params& p, const LDS char* pP, const LDS char* pQ, LDS char* pY, int xf, u32 xn, int yf) {
    f32x4 acc[8];
#pragma unroll
    for (int c = 0; c < 8; ++c)
        if ((M >> c) & 1) { acc[c] = f32x4{0.f, 0.f, 0.f, 0.f}; asm volatile("" : "+v"(acc[c])); }
    Frags F;
    u32 fl = 0;
    first_loads<M, 0, SC<M>.upto[0], true>(pP, pQ, F, fl);
    flag_wait(xf, xn, 0);
    first_loads<M, 0, SC<M>.upto[0], false>(pP, pQ, F, fl);
    conv_mms<M, 0, SC<M>.nm>(p, pP, pQ, pY, F, acc, fl);
    put_y<(M < 4 ? 0 : 3), true>(pY, acc);
    flag_add<0>(yf);
}
__device__ __forceinline__ void conv_wave(const Params& p, LDS char* lds, int w, int lane) {
    LDS char* ch = lds + w * CHSZ;
    const int a = lane & 15, g = lane >> 4, n = a >> 1;
    const LDS char* pP = ch + (a & 1) * PCOPY + 64 * n + 16 * g;
    const LDS char* pQ = ch + PSZ + (a & 1) * QCOPY + 56 - 8 * n + 16 * g;
    LDS char* pY = ch + PSZ + 512 + 4 * ((16 * n + 4 * g + (a & 1)) ^ ((n >> 2) << 3));
    asm volatile("" : "+v"(pY), "+v"(pQ), "+v"(pP));
    __builtin_amdgcn_s_setprio(2);
    conv_run<0xFF>(p, pP, pQ, pY, 0, 4, 7);
}

__device__ __forceinline__ void zero_pads(LDS char* lds, LDS char* ch, int lane) {
    const int h = lane >> 5, l = lane & 31;
    st128(l < 29 ? ch + h * PCOPY + 16 * l : lds + DUMP, u32x4{0, 0, 0, 0});
    st32(l < 16 ? ch + PSZ + h * QCOPY + 4096 + 4 * l : lds + DUMP, 0);
    st16(ch + PSZ + 4094, 0);
}

__device__ __forceinline__ void a_pre(const Params& p, LDS char* lds, int ch0, int w, int lane) {
    LDS char* ch = lds + w * CHSZ;
    auto rs = rsrc(p.ssm_k, SEQ * DCH * 2);
    u32x4 kk[4];
#pragma unroll
    for (int it = 0; it < 4; ++it) kk[it] = __builtin_amdgcn_raw_buffer_load_b128(rs, lane * 16 + it * 1024, (ch0 + w) * 4096, 0);
    bar();
    const float dd = uni16(p.ssm_D, ch0 + w);
    zero_pads(lds, ch, lane);
    const u32 k0 = (kk[0].x & 0xffff0000u) | (pk(bf_lo(kk[0].x) + dd, 0.f) & 0xffffu);
    kk[0].x = lane == 0 ? k0 : kk[0].x;
#pragma unroll
    for (int it = 0; it < 4; ++it) {
        u32 old = it == 0 ? 0u : (u32)__builtin_amdgcn_readlane((int)kk[it - 1].w, 63);
        u32 pw = shr1(old, kk[it].w);
        st128(ch + PCOPY + 448 + (it * 64 + lane) * 16, kk[it]);
        st128(ch + 448 + (it * 64 + lane) * 16, u32x4{pw, kk[it].x, kk[it].y, kk[it].z});
    }
}

struct KV { u32x4 k0, k1, v0, v1, h; };
struct QQ { u32x4 a, b; };

struct Help {
    LDS char *q0, *q3, *q6;
    const LDS char *y0, *y4;
    __device__ __forceinline__ Help(LDS char* lds, int hw, int lane) {
        q0 = lds + PSZ + 4090 - 3072 - 256 * hw - 4 * lane;
        q3 = q0 + 3 * CHSZ;
        q6 = q0 + 6 * CHSZ;
        y0 = lds + PSZ + 512 + ((lane >> 2) & 3) * CHSZ + (hw < 2 ? QCOPY : 0) +
             ((256 * (hw & 1) + 32 * (lane >> 4) + 8 * (lane & 3)) ^ (32 * (hw & 1)));
        y4 = y0 + 4 * CHSZ;
        asm volatile("" : "+v"(q0), "+v"(q3), "+v"(q6), "+v"(y0), "+v"(y4));
    }
    __device__ __forceinline__ void put_x(int c, int s, u32 x) const {
        LDS char* qc = (c < 3 ? q0 : c < 6 ? q3 : q6) + 1024 * (3 - s) + (c % 3) * CHSZ;
        st32(qc + QCOPY + 2, x);
        st16(qc, x);
        st16(qc + 2, x >> 16);
    }
};

__device__ __forceinline__ void a_help(const Params& p, LDS char* lds, int ch0, int hw, int lane) {
    __builtin_amdgcn_s_setprio(3);
    auto rk = rsrc(p.k, SEQ * DCH * 2), rv = rsrc(p.v, SEQ * DCH * 2), rq = rsrc(p.q, SEQ * DCH * 2);
    auto ro = rsrc(p.out, SEQ * DCH * 4);
    const int col = ch0 * 2;
    const int okv = (128 * hw + 2 * lane) * 1280 + col;
    const int oh = lane < 3 ? (128 * hw - 1 - lane) * 1280 + col : 0x7ff00000;
    const int oq = (128 * hw + lane) * 1280 + col;
    auto ld_kv = [&](KV& r, int s, int i) {
        const int so = s * 512 * 1280;
        if (i == 0) r.k0 = __builtin_amdgcn_raw_buffer_load_b128(rk, okv, so, 0);
        if (i == 1) r.k1 = __builtin_amdgcn_raw_buffer_load_b128(rk, okv + 1280, so, 0);
        if (i == 2) r.h = __builtin_amdgcn_raw_buffer_load_b128(rk, oh + so, 0, 0);
        if (i == 3) r.v0 = __builtin_amdgcn_raw_buffer_load_b128(rv, okv, so, 0);
        if (i == 4) r.v1 = __builtin_amdgcn_raw_buffer_load_b128(rv, okv + 1280, so, 0);
    };
    auto ld_q = [&](QQ& r, int s, int i) {
        if (i == 0) r.a = __builtin_amdgcn_raw_buffer_load_b128(rq, oq, s * 512 * 1280, 0);
        if (i == 1) r.b = __builtin_amdgcn_raw_buffer_load_b128(rq, oq + 64 * 1280, s * 512 * 1280, 0);
    };
    KV b0, b1;
    QQ q0, q1, q2, q3;
    asm volatile("s_barrier" ::: "memory");
#pragma unroll
    for (int i = 0; i < 5; ++i) ld_kv(b0, 0, i);
    float wt[8][4];
    {
        cptr sw = (cptr)(p.shift_k + ch0 * 8);
#pragma unroll
        for (int c = 0; c < 8; ++c) {
            const u32 x = sw[2 * c], y = sw[2 * c + 1];
            wt[c][0] = bf_lo(x); wt[c][1] = bf_hi(x); wt[c][2] = bf_lo(y); wt[c][3] = bf_hi(y);
        }
    }
    const Help H(lds, hw, lane);
    auto build = [&](const KV& r, int s, auto&& side) {
#pragma unroll
        for (int d = 0; d < 4; ++d) {
            const u32 h2 = (u32)__builtin_amdgcn_update_dpp(0, (int)r.h[d], 0x101, 0xf, 0xf, true);
            const u32 h3 = (u32)__builtin_amdgcn_update_dpp(0, (int)r.h[d], 0x102, 0xf, 0xf, true);
            const u32 m1 = shr1(r.h[d], r.k1[d]), m2 = shr1(h2, r.k0[d]), m3 = shr1(h3, m1);
#pragma unroll
            for (int e = 0; e < 2; ++e) {
                const int c = 2 * d + e;
                const float k0 = e ? bf_hi(r.k0[d]) : bf_lo(r.k0[d]), k1 = e ? bf_hi(r.k1[d]) : bf_lo(r.k1[d]);
                const float a1 = e ? bf_hi(m1) : bf_lo(m1), a2 = e ? bf_hi(m2) : bf_lo(m2), a3 = e ? bf_hi(m3) : bf_lo(m3);
                const float v0 = e ? bf_hi(r.v0[d]) : bf_lo(r.v0[d]), v1 = e ? bf_hi(r.v1[d]) : bf_lo(r.v1[d]);
                float s0, s1;
                asm("v_mul_f32 %0, %5, %6\n\tv_mul_f32 %1, %5, %7\n\tv_fmac_f32 %0, %4, %7\n\tv_fmac_f32 %1, %4, %8\n\t"
                    "v_fmac_f32 %0, %3, %8\n\tv_fmac_f32 %1, %3, %9\n\tv_fmac_f32 %0, %2, %9\n\tv_fmac_f32 %1, %2, %10\n\t"
                    "v_mul_f32 %0, %0, %11\n\tv_mul_f32 %1, %1, %12"
                    : "=&v"(s0), "=&v"(s1)
                    : "s"(wt[c][0]), "s"(wt[c][1]), "s"(wt[c][2]), "s"(wt[c][3]), "v"(a3), "v"(a2), "v"(a1), "v"(k0), "v"(k1), "v"(v0), "v"(v1));
                u32 x = pk(s1, s0);
                asm volatile("" : "+v"(x));
                H.put_x(c, s, x);
                side(c);
                __builtin_amdgcn_sched_barrier(0x3f7 & ~0x30);
            }
        }
        flag_add(s);
    };
    const int oo = (128 * hw + lane) * 2560 + col;
    auto store = [&](const QQ& qq, int s) {
        float qf[2][8];
#pragma unroll
        for (int i = 0; i < 8; ++i) {
            const u32 x = i < 4 ? qq.a[i & 3] : qq.b[i & 3];
            qf[i >> 2][2 * (i & 3)] = bf_lo(x); qf[i >> 2][2 * (i & 3) + 1] = bf_hi(x);
            asm volatile("" : "+v"(qf[i >> 2][2 * (i & 3)]), "+v"(qf[i >> 2][2 * (i & 3) + 1]));
        }
        int o0 = oo + s * 512 * 2560, o1 = o0 + 64 * 2560;
        asm volatile("" : "+v"(o0), "+v"(o1));
        __builtin_amdgcn_s_setprio(0);
        flag_wait<2>(4 + s, 8, 0);
        __builtin_amdgcn_s_setprio(3);
        u32x2 y[4];
#pragma unroll
        for (int i = 0; i < 4; ++i) y[i] = tr_read((i & 1 ? H.y4 : H.y0) + 128 * (i >> 1) + 1024 * (3 - s));
#pragma unroll
        for (int h = 0; h < 2; ++h) {
            u32x4 o;
#pragma unroll
            for (int d = 0; d < 4; ++d) {
                const u32 yv = y[2 * h + (d >> 1)][d & 1];
                float m0, m1;
                asm("v_mul_f32 %0, %2, %3\n\tv_mul_f32 %1, %4, %5" : "=&v"(m0), "=&v"(m1)
                    : "v"(bf_lo(yv)), "v"(qf[h][2 * d]), "v"(bf_hi(yv)), "v"(qf[h][2 * d + 1]));
                o[d] = pk(m0, m1);
            }
            __builtin_amdgcn_raw_buffer_store_b128(o, ro, h ? o1 : o0, 0, 0);
            __builtin_amdgcn_sched_barrier(0);
        }
    };
    build(b0, 0, [&](int) {});
#pragma unroll
    for (int i = 0; i < 5; ++i) ld_kv(b1, 1, i);
    build(b1, 1, [&](int c) { ld_kv(b0, 2, c); ld_q(q0, 0, c - 5); });
    build(b0, 2, [&](int c) { ld_kv(b1, 3, c); ld_q(q1, 1, c - 5); });
    store(q0, 0);
    build(b1, 3, [&](int c) { ld_q(q2, 2, c); ld_q(q3, 3, c - 2); });
    store(q1, 1);
    store(q2, 2);
    store(q3, 3);
}

typedef f32x2 cf;
#define CMUL(d, a, b) "v_pk_mul_f32 " d ", " a ", " b " op_sel_hi:[1,0]\n\t"
#define CFMA(d, a, b) "v_pk_fma_f32 " d ", " a ", " b ", " d " op_sel:[1,1,0] op_sel_hi:[0,1,1] neg_lo:[1,0,0]\n\t"
__device__ __forceinline__ cf cmul(cf a, cf b) {
    cf r;
    asm(CMUL("%0", "%1", "%2") "s_nop 0\n\t" CFMA("%0", "%1", "%2") : "=&v"(r) : "v"(a), "v"(b));
    return r;
}
__device__ __forceinline__ void cmul2(cf& r0, cf& r1, cf a0, cf b0, cf a1, cf b1) {
    asm(CMUL("%0", "%2", "%3") CMUL("%1", "%4", "%5") CFMA("%0", "%2", "%3") CFMA("%1", "%4", "%5")
        : "=&v"(r0), "=&v"(r1) : "v"(a0), "v"(b0), "v"(a1), "v"(b1));
}
__device__ __forceinline__ cf cexp_l(float a2, float fr, float l) {
    float m = __builtin_amdgcn_exp2f(a2 * l);
    float p = __builtin_amdgcn_fractf(fr * l);
    return cf{m * __builtin_amdgcn_cosf(p), m * __builtin_amdgcn_sinf(p)};
}

struct Tab {
    LDS char* wr[8];
    const LDS char* rd[4];
    __device__ __forceinline__ Tab(LDS char* scr, int lane) {
#pragma unroll
        for (int r = 0; r < 8; ++r) { wr[r] = scr + ((4 * lane + 32 * r) & 255); asm volatile("" : "+v"(wr[r])); }
#pragma unroll
        for (int k = 0; k < 4; ++k) { rd[k] = scr + 256 * (lane & 15) + ((64 * k + (lane & 48) + 32 * lane) & 255); asm volatile("" : "+v"(rd[k])); }
    }
};
__device__ __forceinline__ void kgen(float dt, const Tab& T, const u32 (&raw)[6], f32x4 (&D)[3]) {
    const float a2 = (dt * bf_lo(raw[0])) * 1.4426950408889634f, fr = (dt * bf_lo(raw[1])) * 0.15915494309189535f;
    const cf E1 = cexp_l(a2, fr, 1.f), E64 = cexp_l(a2, fr, 64.f);
    cf W, E2, E4, E8, E128, E256, E512;
    cmul2(W, E2, cf{bf_lo(raw[4]), bf_lo(raw[5])}, cf{dt * bf_lo(raw[2]), dt * bf_lo(raw[3])}, E1, E1);
    cmul2(E4, E128, E2, E2, E64, E64);
    cmul2(E8, E256, E4, E4, E128, E128);
    auto put = [&](int buf, int r, u32 x, u32 y) {
        asm volatile("ds_write2st64_b32 %0, %1, %2 offset0:%3 offset1:%4"
                     :: "v"(T.wr[r]), "v"(x), "v"(y), "i"(18 * buf + r), "i"(18 * buf + r + 8) : "memory");
    };
    auto put2 = [&](int buf, int r, cf x, cf y) { put(buf, r, pk(x.x, x.y), pk(y.x, y.y)); };
    cf X[8];
    X[0] = W;
    {
        cf xa = E8, xb;
        put(0, 0, 0x3f80u, pk(E64.x, -E64.y));
        cmul2(xb, X[1], E64, E8, W, E128);
#pragma unroll
        for (int j = 1; j < 8; ++j) { put(0, j, pk(xa.x, -xa.y), pk(xb.x, -xb.y)); if (j < 7) cmul2(xa, xb, xa, E8, xb, E8); }
    }
    u32x4 Vf[4];
#pragma unroll
    for (int kk = 0; kk < 4; ++kk) Vf[kk] = ld128(T.rd[kk]);
    {
        cf xa = X[0], xb = X[1];
#pragma unroll
        for (int b = 0; b < 8; ++b) { put2(1, b, xa, xb); if (b < 7) cmul2(xa, xb, xa, E1, xb, E1); }
    }
#pragma unroll
    for (int i = 2; i < 8; i += 2) cmul2(X[i], X[i + 1], X[i - 2], E256, X[i - 1], E256);
    E512 = cmul(E256, E256);
#pragma unroll
    for (int kk = 0; kk < 4; ++kk) D[0] = mfma(ld128(T.rd[kk] + 4608), Vf[kk], D[0]);
    {
        cf xa = X[2], xb = X[3];
#pragma unroll
        for (int b = 0; b < 4; ++b) {
            cf ya = X[4 + (b & 2)], yb = X[5 + (b & 2)];
            if (b & 1) cmul2(ya, yb, ya, E4, yb, E4);
            put2(0, b, xa, ya);
            put2(0, 4 + b, xb, yb);
            if (b < 3) cmul2(xa, xb, xa, E2, xb, E2);
        }
    }
#pragma unroll
    for (int kk = 0; kk < 4; ++kk) D[1] = mfma(ld128(T.rd[kk]), Vf[kk], D[1]);
    {
#pragma unroll
        for (int i = 0; i < 2; ++i) {
            cf xa, xb, ya, yb;
            cmul2(xa, xb, X[4 + i], E512, X[6 + i], E512);
            cmul2(ya, yb, xa, E512, xb, E512);
            put2(1, 4 * i, xa, xb);
            put2(1, 4 * i + 1, ya, yb);
            if (i == 0) { const cf xc = cmul(ya, E512); put2(1, 2, xc, xc); }
        }
    }
#pragma unroll
    for (int kk = 0; kk < 4; ++kk) D[2] = mfma(ld128(T.rd[kk] + 4608), Vf[kk], D[2]);
}

__device__ __forceinline__ u32 perm(u32 lo, u32 hi, u32 sel) { return __builtin_amdgcn_perm(hi, lo, sel); }

struct Expand {
    LDS char *pb, *pc, *pc2, *pc3;
    u32 s1, s2, s3, s4, s5;
    __device__ __forceinline__ Expand(LDS char* lds, LDS char* ch, int lane) {
        const int j = lane & 15, G = lane >> 4;
        const bool lo = G < 2;
        pb = ch + 448 + 256 * (G >> 1) + 16 * j + 8 * (G & 1);
        pc = ch + 452 + 512 + 16 * lane;
        pc2 = lo ? lds + DUMP : pc + 512;
        pc3 = lo ? lds + DUMP : pc + 512 + PCOPY - 4;
        s1 = lo ? 0x05040302 : 0x03020100; s2 = lo ? 0x05040504 : 0x03020302; s3 = lo ? 0x05040302 : 0x05040504;
        s4 = (j & 1) ? 0x05040504 : 0x05040100; s5 = (j & 1) ? 0x07060706 : 0x07060302;
        asm volatile("" : "+v"(pb), "+v"(pc), "+v"(pc2), "+v"(pc3), "+v"(s1), "+v"(s2), "+v"(s3), "+v"(s4), "+v"(s5));
    }
    static __device__ __forceinline__ u32 shl1(u32 last, u32 src) {
        return (u32)__builtin_amdgcn_update_dpp((int)last, (int)src, 0x130, 0xf, 0xf, false);
    }
    __device__ __forceinline__ void run(const f32x4 (&D)[3]) const {
        const u32 c01 = pk(D[1][0], D[1][1]), c23 = pk(D[1][2], D[1][3]), a01 = pk(D[2][0], D[2][1]), a2 = pk(D[2][2], 0.f);
        const u32 d0 = pk(D[0][0], D[0][1]), d1 = pk(D[0][2], D[0][3]);
        st64(pb + PCOPY, d0, d1);
        st32(pb + 4, d0); st32(pb + 8, d1);
        const u32 a0 = __builtin_amdgcn_readfirstlane(a01);
        const u32 bc0 = shl1(__builtin_amdgcn_readlane(c23, 32), c01), bc1 = shl1(a0, c23);
        const u32 ba = shl1((a0 >> 16) | (__builtin_amdgcn_readfirstlane(a2) << 16), a01);
        auto chunk = [&](LDS char* P0, LDS char* P1, u32x4 v) {
            st128(P1, v);
            st32(P0, v.x); st32(P0 + 4, v.y); st32(P0 + 8, v.z); st32(P0 + 12, v.w);
        };
        chunk(pc, pc + PCOPY - 4, u32x4{perm(c01, c01, 0x01000100), perm(c01, c23, s1), perm(c01, c23, s2), perm(c23, bc0, s3)});
        chunk(pc2, pc3, u32x4{perm(c23, c23, 0x01000100), c23, perm(c23, c23, 0x03020302), perm(bc1, bc1, 0x01000100)});
        const u32 aa0 = perm(a01, a01, 0x01000100), aa1 = perm(a01, a01, 0x03020302);
        chunk(pc + 1536, pc + 1536 + PCOPY - 4, u32x4{aa0, aa0, perm(a01, ba, s4), perm(ba, ba, 0x01000100)});
        chunk(pc + 2560, pc + 2560 + PCOPY - 4, u32x4{aa1, aa1, perm(a01, ba, s5), perm(ba, ba, 0x03020302)});
    }
};

__device__ __forceinline__ void b_main(const Params& p, LDS char* lds, int ch0, int w, int lane) {
    const int cw = w & 3, c = ch0 + cw;
    u32 raw[6];
    {
        const u32 o = 2 * (c * NST + 16 * (w & 12) + lane);
        raw[0] = *(g16)(p.Are + o); raw[1] = *(g16)(p.Aim + o); raw[2] = *(g16)(p.Bre + o);
        raw[3] = *(g16)(p.Bim + o); raw[4] = *(g16)(p.Cre + o); raw[5] = *(g16)(p.Cim + o);
    }
    bar();
    const float dskip = uni16(p.Dskip, c);
    const float dt = bf_lo(pk(expf(uni16(p.log_dt, c)), 0.f));
    const Tab T(lds + (w < 4 ? w * CHSZ + 480 : SCR + (w - 4) * SCRSZ), lane);
    const Expand ex(lds, lds + cw * CHSZ, lane);
    LDS char* part = lds + SCR + cw * SCRSZ + lane * 16;
    if (w < 4) zero_pads(lds, lds + w * CHSZ, lane);
    asm volatile("" :: "v"(dt));
    if (w >= 8) __builtin_amdgcn_s_setprio(2); else if (w >= 4) __builtin_amdgcn_s_setprio(1);
    __builtin_amdgcn_s_waitcnt(0x0F70);
    auto ru = rsrc(p.u, SEQ * DCH * 2);
    auto ro = rsrc(p.out, SEQ * DCH * 4);
    const int col = ch0 * 2;
    u32x2 ub[4][2];
    const int ou = (128 * cw + 2 * lane) * 1280 + col;
    auto load_u = [&](int s) {
        ub[s][0] = __builtin_amdgcn_raw_buffer_load_b64(ru, ou, s * 512 * 1280, 0);
        ub[s][1] = __builtin_amdgcn_raw_buffer_load_b64(ru, ou + 1280, s * 512 * 1280, 0);
    };
    if (w >= 8) load_u(0);
    f32x4 D[3] = {{lane == 0 && w < 4 ? dskip : 0.f, 0.f, 0.f, 0.f}, {0.f, 0.f, 0.f, 0.f}, {0.f, 0.f, 0.f, 0.f}};
    kgen(dt, T, raw, D);
    if (w < 8) {
        __builtin_amdgcn_s_setprio(3);
        flag_wait(8 + cw, w < 4 ? 2 : 1, 0);
#pragma unroll
        for (int t = 0; t < 3; ++t) D[t] += *(const LDS f32x4*)(part + (w < 4 ? 0 : 4 * SCRSZ) + t * 1024);
        if (w < 4) {
            __builtin_amdgcn_s_waitcnt(0x0F70);
            ex.run(D);
            return;
        }
    }
#pragma unroll
    for (int t = 0; t < 3; ++t) *(LDS f32x4*)(part + (w < 8 ? 0 : 4 * SCRSZ) + t * 1024) = D[t];
    flag_add(8 + cw);
    if (w < 8) finish(p);
    __builtin_amdgcn_s_setprio(3);
    const Help H(lds, cw, lane);
    auto build = [&](int s) {
#pragma unroll
        for (int c = 0; c < 4; ++c) H.put_x(c, s, perm(ub[s][1][c >> 1], ub[s][0][c >> 1], (c & 1) ? 0x07060302 : 0x05040100));
        flag_add(s);
    };
    build(0);
    load_u(1); load_u(2); load_u(3);
    build(1); build(2); build(3);
    const int oo = (128 * cw + lane) * 2560 + 1280 + col;
#pragma unroll 1
    for (int s = 0; s < 4; ++s) {
        __builtin_amdgcn_s_setprio(0);
        flag_wait<2>(4 + s, 4, 0);
        __builtin_amdgcn_s_setprio(3);
#pragma unroll
        for (int h = 0; h < 2; ++h)
            __builtin_amdgcn_raw_buffer_store_b64(tr_read(H.y0 + 128 * h + 1024 * (3 - s)), ro, oo + h * 64 * 2560 + s * 512 * 2560, 0, 0);
    }
    finish(p);
}

__global__ __launch_bounds__(832) void h3s4_kernel(Params) {
    __shared__ __attribute__((aligned(16))) char smem[LDS_BYTES];
    LDS char* lds = (LDS char*)smem;
    const int w = __builtin_amdgcn_readfirstlane(threadIdx.x >> 6), lane = threadIdx.x & 63;
    const u32 xcd = blockIdx.x & 7, slot = blockIdx.x >> 3;
    typedef __attribute__((ext_vector_type(8))) u64 u64x8;
    u64x8 ka, kb;
    asm volatile("s_load_dwordx16 %0, %2, 0x0\n\ts_load_dwordx16 %1, %2, 0x40"
                 : "=&s"(ka), "=&s"(kb) : "s"(__builtin_amdgcn_kernarg_segment_ptr()));
    asm volatile("ds_write_b32 %0, %1" :: "v"(FLG + 4 * min(threadIdx.x, 16u)), "v"(0u) : "memory");
    const u32 ex = (slot + 8) >> 5, m = ex & ~(xcd >> 2);
    u32 role = (((slot + 24) >> 5) & (m - 1)) | ((slot >> 1) & 2 & -m);
    u32 ch0 = 64 * xcd + ex * (512 - 32 * xcd - 128 * (xcd >> 2)) + (((8 * slot - 32 * (role & 1) * (2 + 4 * ex)) >> (role & 1)) & 63);
    asm volatile("s_waitcnt lgkmcnt(0)" : "+s"(ka), "+s"(kb), "+s"(ch0), "+s"(role));
    const Params p = {ka[0], ka[1], ka[2], ka[3], ka[4], ka[5], ka[6], ka[7], kb[0], kb[1], kb[2], kb[3], kb[4], kb[5], kb[6], kb[7]};
    if (w == 12) {
        warm_l2(p, lane);
        asm volatile("s_waitcnt vmcnt(0)" ::: "memory");
        finish(p);
    }
    if (role == 0) {
        if (w >= 8) { a_help(p, lds, ch0, w - 8, lane); finish(p); }
        a_pre(p, lds, ch0, w, lane);
    } else if (role == 1) {
        b_main(p, lds, ch0, w, lane);
    } else {
        finish(p);
    }
    conv_wave(p, lds, w, lane);
    finish(p);
}

extern "C" void kernel_launch(void* const* d_in, const int* in_sizes, int n_in,
                              void* d_out, int out_size, void* d_ws, size_t ws_size,
                              hipStream_t stream) {
    Params p;
    u64* a = &p.q;
    for (int i = 0; i < 15; ++i) a[i] = (u64)d_in[i];
    p.out = (u64)d_out;
    h3s4_kernel<<<256, 832, 0, stream>>>(p);
}
```
